```python
import torch

def _softmax(x, axis=-1):
    return torch.softmax(x, dim=axis)

def _gelu(x):
    return torch.nn.functional.gelu(x, approximate='tanh')

def _silu(x):
    return torch.nn.functional.silu(x)

def _sigmoid(x):
    return torch.sigmoid(x)

def _layernorm(x, axis=-1, eps=1e-05):
    m = x.mean(dim=axis, keepdim=True)
    v = x.var(dim=axis, keepdim=True, unbiased=False)
    return (x - m) / torch.sqrt(v + eps)

def _scatter_add(tgt, idx, src, axis=0):
    return tgt.clone().index_add_(axis, idx, src)

def _segment_sum(src, seg, n):
    out = torch.zeros((n,) + src.shape[1:], dtype=src.dtype)
    return out.index_add_(0, seg, src)

def _pad(x, pw):
    flat = tuple((v for pair in reversed(pw) for v in pair))
    return torch.nn.functional.pad(x, flat)
kernel_name = 'adaLN_cond__memory_aug_recurrent_mem_tr__moe_arch_mod__prompt'

def setup_inputs():

    _w = lambda a: (lambda t: t.to(torch.bfloat16) if t.is_floating_point() else t)(torch.as_tensor(a))

    def _A_setup():
        B, S, Dm = (1, 2048, 512)
        H, Dh = (32, 128)
        M, seg = (8, 32)
        seg = min(seg, S)
        S = S // seg * seg
        return {'x': _w((lambda *s: torch.randn(*s, dtype=torch.bfloat16) * 0.05)(B, S, Dm)), 'mem0': _w((lambda *s: torch.randn(*s, dtype=torch.bfloat16) * 0.4)(B, M, Dm)), 'Wq': _w((lambda *s: torch.randn(*s, dtype=torch.bfloat16) * 3.2)(Dm, H, Dh) * Dm ** (-0.5)), 'Wk': _w((lambda *s: torch.randn(*s, dtype=torch.bfloat16) * 3.2)(Dm, H, Dh) * Dm ** (-0.5)), 'Wv': _w((lambda *s: torch.randn(*s, dtype=torch.bfloat16) * 0.4)(Dm, H, Dh) * Dm ** (-0.5)), 'Wo': _w((lambda *s: torch.randn(*s, dtype=torch.bfloat16) * 0.4)(H, Dh, Dm) * (H * Dh) ** (-0.5)), 'seg_len': seg}
    _A_in = _A_setup()

    def _B_setup():
        B, S, D, F, C = (1, 2048, 512, 1376, 2)
        return {'x': _w((lambda *s: torch.randn(*s, dtype=torch.bfloat16) * 0.05)(B, S, D)), 'w_r': _w((lambda *s: torch.randn(*s, dtype=torch.bfloat16) * 0.05)(D)), 'w_gate': _w((lambda *s: torch.randn(*s, dtype=torch.bfloat16) * 0.05)(D, F)), 'w_up': _w((lambda *s: torch.randn(*s, dtype=torch.bfloat16) * 0.05)(D, F)), 'w_down': _w((lambda *s: torch.randn(*s, dtype=torch.bfloat16) * 0.05)(F, D))}
    _B_in = _B_setup()
    return {'A_x': _A_in['x'], 'A_mem0': _A_in['mem0'], 'A_Wq': _A_in['Wq'], 'A_Wk': _A_in['Wk'], 'A_Wv': _A_in['Wv'], 'A_Wo': _A_in['Wo'], 'A_seg_len': _A_in['seg_len'], 'B_x': _B_in['x'], 'B_w_r': _B_in['w_r'], 'B_w_gate': _B_in['w_gate'], 'B_w_up': _B_in['w_up'], 'B_w_down': _B_in['w_down'], 'c': _w((lambda *s: torch.randn(*s, dtype=torch.bfloat16) * 0.05)(1, 512) * 8.0), 'w_mod': _w((lambda *s: torch.randn(*s, dtype=torch.bfloat16) * 0.05)(512, 6 * 512) * 512 ** (-0.5) * 8.0)}

def reference(A_x, A_mem0, A_Wq, A_Wk, A_Wv, A_Wo, A_seg_len, B_x, B_w_r, B_w_gate, B_w_up, B_w_down, c, w_mod):

    def _A_ref(x, mem0, Wq, Wk, Wv, Wo, seg_len):
        B, S, Dm = x.shape
        H, Dh = (Wq.shape[1], Wq.shape[2])
        M = mem0.shape[1]
        scale = Dh ** (-0.5)
        n_seg = S // seg_len
        L = M + seg_len
        idx = torch.arange(L)
        mask = (idx[:, None] < M) | (idx[:, None] >= idx[None, :])
        mem = mem0
        outs = []
        for s in range(n_seg):
            xs = x[:, s * seg_len:(s + 1) * seg_len, :].clone()
            h = torch.concatenate([mem, xs], axis=1)
            q = torch.einsum('btd,dhe->bhte', h, Wq)
            k = torch.einsum('btd,dhe->bhte', h, Wk)
            v = torch.einsum('btd,dhe->bhte', h, Wv)
            scores = torch.einsum('bhqe,bhke->bhqk', q, k) * scale
            scores = torch.where(mask[None, None], scores, -1000000000.0)
            p = _softmax(scores, axis=-1)
            o = torch.einsum('bhqk,bhke->bhqe', p, v)
            o = torch.einsum('bhte,hed->btd', o, Wo)
            mem = o[:, :M, :].clone()
            outs.append(o[:, M:, :])
        return torch.concatenate(outs, axis=1)

    def _B_ref(x, w_r, w_gate, w_up, w_down):
        B, S, D = x.shape
        C = 2
        r = x @ w_r
        idx = torch.argsort(-r, axis=-1)[:, :C]
        idx_e = (lambda x, n, axis: torch.repeat_interleave(x, n, dim=axis))(idx.reshape(B, C, 1), D, axis=2)
        x_sel = (lambda x, i, axis: torch.take_along_dim(x, i, dim=axis))(x, idx_e, axis=1)
        g = _silu(x_sel @ w_gate)
        u = x_sel @ w_up
        y = g * u @ w_down
        r_sel = _sigmoid((lambda x, i, axis: torch.take_along_dim(x, i, dim=axis))(r, idx, axis=1))
        y = y * r_sel.reshape(B, C, 1)
        b_off = (torch.arange(B) * S).reshape(B, 1)
        flat_idx = (idx + b_off).reshape(B * C)
        zero = (x * 0.0).reshape(B * S, D)
        upd = _scatter_add(zero, flat_idx, y.reshape(B * C, D), axis=0)
        return x + upd.reshape(B, S, D)
    x = A_x
    mod = (_silu(c) @ w_mod).reshape(c.shape[0], 6, 1, -1)
    sa, ca, ga, sb, cb, gb = (mod[:, 0].clone(), mod[:, 1].clone(), mod[:, 2].clone(), mod[:, 3].clone(), mod[:, 4].clone(), mod[:, 5].clone())
    h = _layernorm(x, axis=-1) * (1 + ca) + sa
    x = x + ga * _A_ref(h, A_mem0, A_Wq, A_Wk, A_Wv, A_Wo, A_seg_len)
    h = _layernorm(x, axis=-1) * (1 + cb) + sb
    x = x + gb * _B_ref(h, B_w_r, B_w_gate, B_w_up, B_w_down)
    return x
```

```cpp
#include <hip/hip_runtime.h>
#include <hip/hip_bf16.h>
#include <stdint.h>

namespace {
typedef unsigned short u16;
typedef unsigned int u32;
typedef unsigned long long u64;
typedef u32 u32x2 __attribute__((ext_vector_type(2)));
typedef u32 u32x4 __attribute__((ext_vector_type(4)));
typedef u32 u32x8 __attribute__((ext_vector_type(8)));
typedef float f32x2 __attribute__((ext_vector_type(2)));
typedef float f32x4 __attribute__((ext_vector_type(4)));
typedef short s16x4 __attribute__((ext_vector_type(4)));
typedef __bf16 bf16x2 __attribute__((ext_vector_type(2)));
typedef __bf16 bf16x8 __attribute__((ext_vector_type(8)));
typedef __amdgpu_buffer_rsrc_t rsrc_t;
#define LAS __attribute__((address_space(3)))
#define DEV __device__ __forceinline__

constexpr int NROW = 2048, DM = 512, DH = 128, FF = 1376;
constexpr u32 SENT = 0x7F817F81u;
constexpr u32 SENTM = 0xFFFFFFFFu;
constexpr u32 SPINCAP = 1u << 19;
constexpr int SC1 = 16;

constexpr u32 PAGE = 2u << 20;
constexpr u32 WS_EXCH = 0;
constexpr u32 WS_CTL = 8 * PAGE;
constexpr u32 CT_FKV = WS_CTL, CT_FQ = WS_CTL + 8192, CT_FO = WS_CTL + 10240, CT_FM = WS_CTL + 32768,
              CT_TOP = WS_CTL + 131072, WS_ACT = WS_CTL + 163840;
constexpr u32 CTL_BYTES = 180224;
constexpr u32 CT_EPOCH = WS_CTL + CTL_BYTES, CT_FP = CT_EPOCH + 256, CT_FI = CT_EPOCH + 8192, CT_FH = CT_FI + 8192,
              WS_MOD = CT_FH + 8192;
constexpr u32 WS_MODP = WS_MOD + 16384;
constexpr u32 TAGADD = 0x9E3779B9u;
constexpr u32 WS_UA = 10 * PAGE;
constexpr u32 WS_UB = WS_UA + 18 * PAGE;
constexpr u32 WS_UC = WS_UB + 18 * PAGE;
constexpr u32 WS_H = WS_UC + 18 * PAGE;
constexpr u32 WS_H2 = WS_H + 2 * PAGE;
constexpr u32 WS_X1 = WS_H2 + 2 * PAGE;
constexpr u32 WS_DBG = WS_X1 + 2 * PAGE;
constexpr u32 WS_END = WS_DBG + (2u << 20);

DEV u32 pk2(float lo, float hi) { f32x2 f = {lo, hi}; return __builtin_bit_cast(u32, __builtin_convertvector(f, bf16x2)); }
DEV float bfr(float x) { return __uint_as_float(pk2(x, 0.f) << 16); }
DEV float bflo(u32 v) { return __uint_as_float(v << 16); }
DEV float bfhi(u32 v) { return __uint_as_float(v & 0xFFFF0000u); }
DEV float bfel(u32x4 v, int j) { return (j & 1) ? bfhi(v[j >> 1]) : bflo(v[j >> 1]); }
DEV f32x4 mfma32(u32x4 a, u32x4 b, f32x4 c) { return __builtin_amdgcn_mfma_f32_16x16x32_bf16(__builtin_bit_cast(bf16x8, a), __builtin_bit_cast(bf16x8, b), c, 0, 0, 0); }
DEV u32x4 half4(u32x8 v, int hi) { return hi ? __builtin_shufflevector(v, v, 4, 5, 6, 7) : __builtin_shufflevector(v, v, 0, 1, 2, 3); }
DEV u32x4 pk8(f32x4 a, f32x4 c) { return (u32x4){pk2(a[0], a[1]), pk2(a[2], a[3]), pk2(c[0], c[1]), pk2(c[2], c[3])}; }
DEV u32x2 trd(u32 a) { return __builtin_bit_cast(u32x2, __builtin_amdgcn_ds_read_tr16_b64_v4i16((LAS s16x4*)(uintptr_t)a)); }
DEV u32x4 lds_rd128(u32 a) { return *(LAS u32x4*)(uintptr_t)a; }
DEV u32x4 lds_rd128o(LAS char* base, int off) { return *(LAS u32x4*)(base + off); }
#define SCHED() __builtin_amdgcn_sched_barrier(0)
DEV u32x2 lds_rd64(u32 a) { return *(LAS u32x2*)(uintptr_t)a; }
DEV u32 lds_rd32(u32 a) { return *(LAS u32*)(uintptr_t)a; }
DEV void lds_wr128(u32 a, u32x4 v) { *(LAS u32x4*)(uintptr_t)a = v; }
DEV void lds_wr64(u32 a, u32x2 v) { *(LAS u32x2*)(uintptr_t)a = v; }
DEV void lds_wr32(u32 a, u32 v) { *(LAS u32*)(uintptr_t)a = v; }
DEV void bar() { asm volatile("s_waitcnt lgkmcnt(0)\n\ts_barrier" ::: "memory"); }
#define VMWAIT(n) asm volatile("s_waitcnt vmcnt(" #n ")" ::: "memory")
DEV float xmax16(float v) { auto r = __builtin_amdgcn_permlane16_swap(__float_as_uint(v), __float_as_uint(v), false, false); return fmaxf(__uint_as_float(r[0]), __uint_as_float(r[1])); }
DEV float xmax32(float v) { auto r = __builtin_amdgcn_permlane32_swap(__float_as_uint(v), __float_as_uint(v), false, false); return fmaxf(__uint_as_float(r[0]), __uint_as_float(r[1])); }
DEV float xsum16(float v) { auto r = __builtin_amdgcn_permlane16_swap(__float_as_uint(v), __float_as_uint(v), false, false); return __uint_as_float(r[0]) + __uint_as_float(r[1]); }
DEV float xsum32(float v) { auto r = __builtin_amdgcn_permlane32_swap(__float_as_uint(v), __float_as_uint(v), false, false); return __uint_as_float(r[0]) + __uint_as_float(r[1]); }
template <int CTRL> DEV float dppf(float v) { return __builtin_amdgcn_update_dpp(0.f, v, CTRL, 0xf, 0xf, true); }
DEV float wave_sum(float v) { v += dppf<0x121>(v); v += dppf<0x122>(v); v += dppf<0x124>(v); v += dppf<0x128>(v); return xsum32(xsum16(v)); }
DEV u32 nosent(u32 v) { return min(v, 0xFFFFFFFEu); }
DEV bool hassent(u32x4 v) { return v[0] == SENT || v[1] == SENT || v[2] == SENT || v[3] == SENT; }
DEV rsrc_t mkrsrc(const void* p, u32 bytes) { return __builtin_amdgcn_make_buffer_rsrc((void*)p, 0, bytes, 0x00020000); }
DEV u32x4 ld128(rsrc_t r, u32 vo, u32 so) { return __builtin_amdgcn_raw_buffer_load_b128(r, vo, so, SC1); }
DEV u32x2 ld64(rsrc_t r, u32 vo, u32 so) { return __builtin_amdgcn_raw_buffer_load_b64(r, vo, so, SC1); }
DEV u32 ld32(rsrc_t r, u32 vo, u32 so) { return __builtin_amdgcn_raw_buffer_load_b32(r, vo, so, SC1); }
DEV void st128(u32x4 v, rsrc_t r, u32 vo, u32 so) { __builtin_amdgcn_raw_buffer_store_b128(v, r, vo + so, 0, SC1); }
DEV void st64(u32x2 v, rsrc_t r, u32 vo, u32 so) { __builtin_amdgcn_raw_buffer_store_b64(v, r, vo, so, SC1); }
DEV void st32(u32 v, rsrc_t r, u32 vo, u32 so) { __builtin_amdgcn_raw_buffer_store_b32(v, r, vo, so, SC1); }
DEV void glds16(const void* gsrc, u32 lds_dst) {
  u32 keep;
  asm volatile("s_mov_b32 %0, m0\n\ts_mov_b32 m0, %2\n\ts_nop 0\n\tglobal_load_lds_dwordx4 %1, off\n\ts_mov_b32 m0, %0" : "=&s"(keep) : "v"(gsrc), "s"(lds_dst) : "memory");
}
DEV void glds16c(const void* gsrc, u32 lds_dst) {
  u32 keep;
  asm volatile("s_mov_b32 %0, m0\n\ts_mov_b32 m0, %2\n\ts_nop 0\n\tglobal_load_lds_dwordx4 %1, off sc1\n\ts_mov_b32 m0, %0" : "=&s"(keep) : "v"(gsrc), "s"(lds_dst) : "memory");
}
DEV u32 nearh(u32 par, u32 page) { return ((par ^ page) & 1u) << 13; }
DEV u32 farh(u32 par, u32 page) { return ((par ^ page ^ 1u) & 1u) << 13; }
DEV u32 rowoff(u32 par, u32 row) { return (row >> 3) * 16384 + farh(par, row >> 10) + (row & 7) * 1024; }
DEV u32 chunk_off(int c) { return ((u32)(c >> 3) << 18) | ((u32)((c & 7) + 1) << 14); }
#define HPX 1
#define LASTSLEEP 6
#define FARSLEEP 40
#define FMNEAR 1
#define ACTSLEEP 20
#define GOSLEEP 16
#define LW1 48
#define FOSLEEP 3
#define POLLSLEEP 6
template <int N> DEV void wait_flags(rsrc_t r, u32 off, u32 stride, u32 need, int lane) {
  for (u32 sp = 0; sp < SPINCAP; ++sp) {
    asm volatile("" ::: "memory");
    const u32 v = ld32(r, (lane & (N - 1)) * stride, off);
    if (__all(v >= need)) break;
    __builtin_amdgcn_s_sleep(POLLSLEEP);
  }
  asm volatile("" ::: "memory");
}

DEV u32 mktag(u32 ev) { asm volatile("" : "+v"(ev)); return ev + TAGADD; }
template <bool NEEDH> DEV u32 wait_start(rsrc_t r, u32 tag, int lane, int hmask = 63) {
  u32x2 fp = {0, 0};
  tag = mktag(tag);
  for (u32 sp = 0; sp < SPINCAP; ++sp) {
    asm volatile("" ::: "memory");
    const u32 a = ld32(r, lane * 128, CT_FI), b = NEEDH ? ld32(r, (lane & hmask) * 128, CT_FH) : tag;
    fp = ld64(r, 0, CT_FP);
    if (__all(a == tag && b == tag && fp[1] == tag)) break;
    __builtin_amdgcn_s_sleep(2);
  }
  asm volatile("" ::: "memory");
  return __builtin_amdgcn_readfirstlane(fp[0]) & 1u;
}
template <int N> DEV void mod_gather(rsrc_t r, u32 tag, u32 off, u32 dst, int t) {
  u32x4 v[N];
  tag = mktag(tag);
  for (u32 sp = 0; sp < SPINCAP; ++sp) {
    asm volatile("" ::: "memory");
    bool ok = true;
#pragma unroll
    for (int i = 0; i < N; ++i) v[i] = ld128(r, t * 16 + i * 4096, WS_MOD + off);
#pragma unroll
    for (int i = 0; i < N; ++i) ok = ok && v[i][1] == tag && v[i][3] == tag;
    if (__all(ok)) break;
    __builtin_amdgcn_s_sleep(2);
  }
#pragma unroll
  for (int i = 0; i < N; ++i) lds_wr64(dst + t * 8 + i * 2048, (u32x2){v[i][0], v[i][2]});
}
DEV void wait_fm(rsrc_t r, u32 par, int h, u32 need, int lane) {
  const u32 vo = ((lane & 4) + max(lane & 3, 1)) * 128;
  for (u32 sp = 0; sp < SPINCAP; ++sp) {
    asm volatile("" ::: "memory");
    const u32 v = ld32(r, vo, CT_FM + (h >> 3) * 16384 + (FMNEAR ? nearh(par, 0) : 0) + (h & 7) * 1024);
    if (__all(v >= need)) break;
    if (__all(v + 1 < need)) __builtin_amdgcn_s_sleep(FARSLEEP);
    else __builtin_amdgcn_s_sleep(POLLSLEEP);
  }
  asm volatile("" ::: "memory");
}

enum { MAP_NAT = 0, MAP_RQKV = 1, MAP_ACC = 3 };
template <int MAP> DEV int rowmap(int sg) {
  const int n = sg >> 4, gB = (sg >> 2) & 3, q = sg & 3;
  const int L = 32 * (n >> 1) + 8 * gB + 4 * (n & 1) + q;
  if (MAP == MAP_NAT) return L;
  if (MAP == MAP_ACC) return 32 * (n >> 1) + 16 * (n & 1) + 4 * gB + q;
  const int g = L >> 4, p = (L >> 2) & 3, u = (L >> 1) & 1, b = L & 1;
  return 16 * p + 8 * (g >> 1) + 4 * u + 2 * (g & 1) + b;
}
template <int NT> DEV int swz(int sg) { return NT == 8 ? (sg & 7) : (NT == 4 ? ((sg >> 1) & 3) : 0); }
template <int NT, int MAP, int CNT> DEV void slab_dma(const u16* g, int ld, u32 lds_slab, int I0, int lane) {
#pragma unroll
  for (int i = 0; i < CNT; ++i) {
    const int I = I0 + i, sg = I * (32 / NT) + lane / (2 * NT), kap = (lane >> 1) & (NT - 1), half = lane & 1;
    glds16(g + (size_t)rowmap<MAP>(sg) * ld + ((kap ^ swz<NT>(sg)) * 16 + half * 8), lds_slab + I * 1024);
  }
}
template <int NT> DEV u32x8 slab_frag(u32 lds_slab, int tile, int lane) {
  const int sl = lane >> 2;
  const u32 a = lds_slab + sl * (NT * 32) + ((tile ^ swz<NT>(sl)) * 32) + (lane & 3) * 8;
  const u32x2 r0 = trd(a), r1 = trd(a + NT * 512), r2 = trd(a + 2 * NT * 512), r3 = trd(a + 3 * NT * 512);
  return (u32x8){r0[0], r0[1], r1[0], r1[1], r2[0], r2[1], r3[0], r3[1]};
}

#define PFMODE 1
#define NPROBE 12
#define PINV 0
#define TS(k)
#define TSV(k, v)
struct Params {
  const u16 *x, *mem0, *wq, *wk, *wv, *wo, *wr, *wg, *wu, *wd, *c, *wmod;
  u16* out;
  char* ws;
  int dlya, dlyb;
};

DEV void role_init(const Params& p, rsrc_t rw, u32 tag, int a, int t, int lane, int w) {
  if (a == 0) {
    int best0 = 1 << 30, best1 = 1 << 30;
    for (int i = 0; i < NPROBE; ++i) {
      char* ad = p.ws + WS_EXCH + (size_t)w * PAGE + (11u << 14) + ((i ^ w) & 1) * 8192;
      u64 c0, c1; u32 xv = i;
      asm volatile("s_memtime %0\n\ts_waitcnt lgkmcnt(0)\n\tglobal_store_dword %3, %2, off sc1\n\ts_waitcnt vmcnt(0)\n\ts_memtime %1\n\ts_waitcnt lgkmcnt(0)" : "=&s"(c0), "=&s"(c1) : "v"(xv), "v"(ad) : "memory");
      const int d = (int)(c1 - c0);
      if (i & 1) best1 = min(best1, d); else best0 = min(best0, d);
    }
    if (lane == 0) lds_wr32(w * 4, (u32)max(min(best0 - best1, 300), -300));
    bar();
    if (t == 0) {
      const u32x4 v = lds_rd128(0);
      st64((u32x2){(((u32)__builtin_amdgcn_s_memrealtime() - 4096u) & ~1u) | (((int)(v[0] + v[1] + v[2] + v[3]) > 0 ? 1u : 0u) ^ PINV), mktag(tag)}, rw, 0, CT_FP);
      st32(mktag(tag), rw, 0, CT_FI);
    }
    TS(3);
  } else {
    for (int u = a - 1; u < 155; u += 63) {
      if (u == 0) {
#pragma unroll
        for (int k = 0; k < 2; ++k) {
          const int i = t + 256 * k, j = i >> 3, pc = i & 7, t8 = j >> 3, ch = (j >> 2) & 1, rp = j & 3;
          const u16* src = p.mem0 + (2 * rp + (pc & 1)) * 512 + 64 * t8 + 8 * ch + 4 * ((pc >> 1) & 1) + 2 * (pc >> 2);
          const u32x4 v = {nosent(*(const u32*)src), nosent(*(const u32*)(src + 16)), nosent(*(const u32*)(src + 32)), nosent(*(const u32*)(src + 48))};
          st128(v, rw, i * 16, WS_EXCH + chunk_off(0)); st128(v, rw, i * 16, WS_EXCH + chunk_off(0) + 8192);
        }
      } else if (u < 144) {
        const u32 sv = u < 64 ? SENTM : SENT;
        const u32 dst = WS_EXCH + (u < 128 ? (u32)(u >> 6) * PAGE + chunk_off(u & 63) : ((u32)((u - 128) >> 1) << 18) + ((9u + (u & 1)) << 14));
#pragma unroll
        for (int i = 0; i < 4; ++i) st128((u32x4){sv, sv, sv, sv}, rw, t * 16 + i * 4096, dst);
      } else {
#pragma unroll
        for (int i = 0; i < 4; ++i) {
          const u32 o = WS_CTL + (u - 144) * 16384 + i * 4096 + t * 16, q = o - WS_ACT;
          const u32 sv = ((o >= CT_TOP && o < WS_ACT) || q < 43 * 128 || q - 6144 < 43 * 128) ? SENTM : 0u;
          st128((u32x4){sv, sv, sv, sv}, rw, o, 0);
        }
      }
    }
  }
  const int h = a >> 1, hf = a & 1;
  const char* sk = (const char*)(p.wk + h * DH) + ((size_t)hf << 21) + (size_t)(w * 64 + (lane >> 4)) * 8192 + (lane & 15) * 16;
  const char* sv = (const char*)(p.wv + h * DH + hf * 64) + (size_t)(w * 128 + (lane >> 3)) * 8192 + (lane & 7) * 16;
  const char* so = (const char*)(p.wo + (size_t)(h * DH + hf * 64) * DM) + w * 16384 + lane * 16;
  const u32 tg = mktag(tag);
#pragma unroll 1
  for (int i = 0; i < 16; ++i) glds16((a ? sk : sv) + (size_t)i * (a ? 4 : 8) * 8192, 1024 + w * 1024);
  VMWAIT(16);
  bar();
  if (t == 0 && a) st32(tg, rw, 0, CT_FI + a * 128);
  TS(1);
#pragma unroll 1
  for (int i = 0; i < 16; ++i) glds16(sv + (size_t)i * 8 * 8192, 1024 + w * 1024);
#pragma unroll 1
  for (int i = 0; i < 16; ++i) glds16(so + i * 1024, 1024 + w * 1024);
  VMWAIT(0);
  TS(2);
}

#define REC_ASM_TEXT \
  "s_mov_b32 s54, %0\n" \
  "v_mbcnt_lo_u32_b32 v0, -1, 0\n" \
  "v_mbcnt_hi_u32_b32 v0, -1, v0\n" \
  "s_lshl_b32 s74, s54, 8\n" \
  "v_lshl_add_u32 v0, v0, 2, s74\n" \
  "v_add_u32 v0, 0x20000, v0\n" \
  "ds_read_b32 v16, v0 offset:31744\n" \
  "ds_read_b32 v1, v0 offset:20480\n" \
  "ds_read_b32 v2, v0 offset:21504\n" \
  "ds_read_b32 v3, v0 offset:22528\n" \
  "ds_read_b32 v4, v0 offset:23552\n" \
  "ds_read_b32 v5, v0 offset:24576\n" \
  "ds_read_b32 v6, v0 offset:25600\n" \
  "ds_read_b32 v7, v0 offset:26624\n" \
  "ds_read_b32 v8, v0 offset:27648\n" \
  "ds_read_b32 v9, v0 offset:28672\n" \
  "ds_read_b32 v10, v0 offset:29696\n" \
  "ds_read_b32 v11, v0 offset:30720\n" \
  "s_waitcnt lgkmcnt(0)\n" \
  "v_readlane_b32 s20, v16, 0\n" \
  "v_readlane_b32 s21, v16, 1\n" \
  "v_readlane_b32 s22, v16, 2\n" \
  "v_readlane_b32 s23, v16, 3\n" \
  "v_readlane_b32 s24, v16, 4\n" \
  "v_readlane_b32 s25, v16, 5\n" \
  "v_readlane_b32 s26, v16, 6\n" \
  "v_readlane_b32 s27, v16, 7\n" \
  "s_lshl_b32 s75, s54, 12\n" \
  "s_lshl_b32 s96, s54, 11\n" \
  "s_lshl_b32 s73, s54, 14\n" \
  "ds_read_b32 v112, v0 offset:0\n" \
  "ds_read_b32 v113, v0 offset:1024\n" \
  "ds_read_b32 v114, v0 offset:2048\n" \
  "ds_read_b32 v115, v0 offset:3072\n" \
  "ds_read_b32 v116, v0 offset:4096\n" \
  "ds_read_b32 v117, v0 offset:5120\n" \
  "ds_read_b32 v118, v0 offset:6144\n" \
  "ds_read_b32 v119, v0 offset:7168\n" \
  "ds_read_b32 v120, v0 offset:8192\n" \
  "ds_read_b32 v121, v0 offset:9216\n" \
  "ds_read_b32 v122, v0 offset:10240\n" \
  "ds_read_b32 v123, v0 offset:11264\n" \
  "ds_read_b32 v124, v0 offset:12288\n" \
  "ds_read_b32 v125, v0 offset:13312\n" \
  "ds_read_b32 v126, v0 offset:14336\n" \
  "ds_read_b32 v127, v0 offset:15360\n" \
  "s_waitcnt lgkmcnt(0)\n" \
  "v_mov_b32 v83, 0\n" \
  "v_mov_b32 v111, 0\n" \
  "v_readlane_b32 s36, v16, 8\n" \
  "v_readlane_b32 s37, v16, 9\n" \
  "v_readlane_b32 s38, v16, 10\n" \
  "v_readlane_b32 s55, v16, 11\n" \
  "v_readlane_b32 s51, v16, 12\n" \
  "v_readlane_b32 s52, v16, 13\n" \
  "v_readlane_b32 s53, v16, 14\n" \
  "v_readlane_b32 s57, v16, 15\n" \
  "v_readlane_b32 s58, v16, 16\n" \
  "v_readlane_b32 s97, v16, 17\n" \
  "v_readlane_b32 s31, v16, 18\n" \
  "v_readlane_b32 s18, v16, 19\n" \
  "v_readlane_b32 s5, v16, 20\n" \
  "v_readlane_b32 s15, v16, 21\n" \
  "v_readlane_b32 s16, v16, 22\n" \
  "v_readlane_b32 s28, v16, 23\n" \
  "v_readlane_b32 s30, v16, 24\n" \
  "s_mov_b32 s39, 0x20000\n" \
  "s_mov_b32 s60, 0xff00ff00\n" \
  "s_mov_b32 s61, 0xff00ff00\n" \
  "s_mov_b32 s62, 0xc8\n" \
  "s_mov_b32 s98, 0x3e0293ee\n" \
  "s_mov_b32 s99, 0xbe0293ee\n" \
  "v_mov_b32 v20, 0\n" \
  "buffer_load_dword v22, v20, s[36:39], s30 offen sc1\n" \
  "s_memtime s[64:65]\n" \
  "s_nop 0\n" \
  "s_memrealtime s[44:45]\n" \
  "s_nop 0\n" \
  "s_memrealtime s[76:77]\n" \
  "s_nop 0\n" \
  "s_memrealtime s[78:79]\n" \
  "s_nop 0\n" \
  "s_memrealtime s[80:81]\n" \
  "s_nop 0\n" \
  "s_memrealtime s[82:83]\n" \
  "s_nop 0\n" \
  "s_memrealtime s[84:85]\n" \
  "s_nop 0\n" \
  "s_memrealtime s[86:87]\n" \
  "s_nop 0\n" \
  "s_memrealtime s[88:89]\n" \
  "s_nop 0\n" \
  "s_memrealtime s[90:91]\n" \
  "s_nop 0\n" \
  "s_memrealtime s[92:93]\n" \
  "s_nop 0\n" \
  "s_memrealtime s[94:95]\n" \
  "s_nop 0\n" \
  "s_memrealtime s[40:41]\n" \
  "s_nop 0\n" \
  "s_memrealtime s[42:43]\n" \
  "s_mov_b32 s66, 0\n" \
  "s_waitcnt lgkmcnt(0)\n" \
  "s_cmp_eq_u32 s76, s44\n" \
  "s_addc_u32 s66, s66, 0\n" \
  "s_cmp_eq_u32 s78, s44\n" \
  "s_addc_u32 s66, s66, 0\n" \
  "s_cmp_eq_u32 s80, s44\n" \
  "s_addc_u32 s66, s66, 0\n" \
  "s_cmp_eq_u32 s82, s44\n" \
  "s_addc_u32 s66, s66, 0\n" \
  "s_cmp_eq_u32 s84, s44\n" \
  "s_addc_u32 s66, s66, 0\n" \
  "s_cmp_eq_u32 s86, s44\n" \
  "s_addc_u32 s66, s66, 0\n" \
  "s_cmp_eq_u32 s88, s44\n" \
  "s_addc_u32 s66, s66, 0\n" \
  "s_cmp_eq_u32 s90, s44\n" \
  "s_addc_u32 s66, s66, 0\n" \
  "s_cmp_eq_u32 s92, s44\n" \
  "s_addc_u32 s66, s66, 0\n" \
  "s_cmp_eq_u32 s94, s44\n" \
  "s_addc_u32 s66, s66, 0\n" \
  "s_cmp_eq_u32 s40, s44\n" \
  "s_addc_u32 s66, s66, 0\n" \
  "s_cmp_eq_u32 s42, s44\n" \
  "s_addc_u32 s66, s66, 0\n" \
  "s_lshl_b32 s66, s66, 3\n" \
  "s_add_u32 s64, s64, s66\n" \
  "s_sub_u32 s65, s44, s31\n" \
  "s_add_u32 s65, s65, 4\n" \
  "s_lshl_b32 s65, s65, 5\n" \
  "s_mov_b32 s2, 0x15c9\n" \
  "s_mov_b32 s3, 0xbc0\n" \
  "s_mov_b32 s0, s64\n" \
  "s_mov_b32 s1, s65\n" \
  "s_waitcnt vmcnt(17)\n" \
  "s_barrier\n" \
  "v_xor_b32 v12, 0x0, v9\n" \
  "ds_read_b64_tr_b16 v[128:129], v12 offset:0\n" \
  "ds_read_b64_tr_b16 v[130:131], v12 offset:4096\n" \
  "ds_read_b64_tr_b16 v[132:133], v12 offset:8192\n" \
  "ds_read_b64_tr_b16 v[134:135], v12 offset:12288\n" \
  "v_xor_b32 v13, 0x20, v9\n" \
  "ds_read_b64_tr_b16 v[192:193], v13 offset:0\n" \
  "ds_read_b64_tr_b16 v[194:195], v13 offset:4096\n" \
  "ds_read_b64_tr_b16 v[196:197], v13 offset:8192\n" \
  "ds_read_b64_tr_b16 v[198:199], v13 offset:12288\n" \
  "s_waitcnt lgkmcnt(0)\n" \
  "v_xor_b32 v12, 0x4000, v9\n" \
  "ds_read_b64_tr_b16 v[136:137], v12 offset:0\n" \
  "ds_read_b64_tr_b16 v[138:139], v12 offset:4096\n" \
  "ds_read_b64_tr_b16 v[140:141], v12 offset:8192\n" \
  "ds_read_b64_tr_b16 v[142:143], v12 offset:12288\n" \
  "v_xor_b32 v13, 0x4020, v9\n" \
  "ds_read_b64_tr_b16 v[200:201], v13 offset:0\n" \
  "ds_read_b64_tr_b16 v[202:203], v13 offset:4096\n" \
  "ds_read_b64_tr_b16 v[204:205], v13 offset:8192\n" \
  "ds_read_b64_tr_b16 v[206:207], v13 offset:12288\n" \
  "s_waitcnt lgkmcnt(0)\n" \
  "v_xor_b32 v12, 0x8000, v9\n" \
  "ds_read_b64_tr_b16 v[144:145], v12 offset:0\n" \
  "ds_read_b64_tr_b16 v[146:147], v12 offset:4096\n" \
  "ds_read_b64_tr_b16 v[148:149], v12 offset:8192\n" \
  "ds_read_b64_tr_b16 v[150:151], v12 offset:12288\n" \
  "v_xor_b32 v13, 0x8020, v9\n" \
  "ds_read_b64_tr_b16 v[208:209], v13 offset:0\n" \
  "ds_read_b64_tr_b16 v[210:211], v13 offset:4096\n" \
  "ds_read_b64_tr_b16 v[212:213], v13 offset:8192\n" \
  "ds_read_b64_tr_b16 v[214:215], v13 offset:12288\n" \
  "s_waitcnt lgkmcnt(0)\n" \
  "v_xor_b32 v12, 0xc000, v9\n" \
  "ds_read_b64_tr_b16 v[152:153], v12 offset:0\n" \
  "ds_read_b64_tr_b16 v[154:155], v12 offset:4096\n" \
  "ds_read_b64_tr_b16 v[156:157], v12 offset:8192\n" \
  "ds_read_b64_tr_b16 v[158:159], v12 offset:12288\n" \
  "v_xor_b32 v13, 0xc020, v9\n" \
  "ds_read_b64_tr_b16 v[216:217], v13 offset:0\n" \
  "ds_read_b64_tr_b16 v[218:219], v13 offset:4096\n" \
  "ds_read_b64_tr_b16 v[220:221], v13 offset:8192\n" \
  "ds_read_b64_tr_b16 v[222:223], v13 offset:12288\n" \
  "s_waitcnt lgkmcnt(0)\n" \
  "s_waitcnt lgkmcnt(0)\n" \
  "s_barrier\n" \
  "s_mov_b64 s[70:71], s[20:21]\n" \
  "s_add_u32 m0, s75, 0\n" \
  "s_nop 0\n" \
  "global_load_lds_dwordx4 v1, s[70:71]\n" \
  "s_add_u32 m0, s75, 0x400\n" \
  "s_nop 0\n" \
  "global_load_lds_dwordx4 v2, s[70:71]\n" \
  "s_add_u32 m0, s75, 0x800\n" \
  "s_nop 0\n" \
  "global_load_lds_dwordx4 v3, s[70:71]\n" \
  "s_add_u32 m0, s75, 0xc00\n" \
  "s_nop 0\n" \
  "global_load_lds_dwordx4 v4, s[70:71]\n" \
  "s_add_u32 s70, s70, 0x80000\n" \
  "s_addc_u32 s71, s71, 0\n" \
  "s_add_u32 m0, s75, 0x4000\n" \
  "s_nop 0\n" \
  "global_load_lds_dwordx4 v1, s[70:71]\n" \
  "s_add_u32 m0, s75, 0x4400\n" \
  "s_nop 0\n" \
  "global_load_lds_dwordx4 v2, s[70:71]\n" \
  "s_add_u32 m0, s75, 0x4800\n" \
  "s_nop 0\n" \
  "global_load_lds_dwordx4 v3, s[70:71]\n" \
  "s_add_u32 m0, s75, 0x4c00\n" \
  "s_nop 0\n" \
  "global_load_lds_dwordx4 v4, s[70:71]\n" \
  "s_add_u32 s70, s70, 0x80000\n" \
  "s_addc_u32 s71, s71, 0\n" \
  "s_add_u32 m0, s75, 0x8000\n" \
  "s_nop 0\n" \
  "global_load_lds_dwordx4 v1, s[70:71]\n" \
  "s_add_u32 m0, s75, 0x8400\n" \
  "s_nop 0\n" \
  "global_load_lds_dwordx4 v2, s[70:71]\n" \
  "s_add_u32 m0, s75, 0x8800\n" \
  "s_nop 0\n" \
  "global_load_lds_dwordx4 v3, s[70:71]\n" \
  "s_add_u32 m0, s75, 0x8c00\n" \
  "s_nop 0\n" \
  "global_load_lds_dwordx4 v4, s[70:71]\n" \
  "s_add_u32 s70, s70, 0x80000\n" \
  "s_addc_u32 s71, s71, 0\n" \
  "s_add_u32 m0, s75, 0xc000\n" \
  "s_nop 0\n" \
  "global_load_lds_dwordx4 v1, s[70:71]\n" \
  "s_add_u32 m0, s75, 0xc400\n" \
  "s_nop 0\n" \
  "global_load_lds_dwordx4 v2, s[70:71]\n" \
  "s_add_u32 m0, s75, 0xc800\n" \
  "s_nop 0\n" \
  "global_load_lds_dwordx4 v3, s[70:71]\n" \
  "s_add_u32 m0, s75, 0xcc00\n" \
  "s_nop 0\n" \
  "global_load_lds_dwordx4 v4, s[70:71]\n" \
  "s_waitcnt vmcnt(16)\n" \
  "s_barrier\n" \
  "v_xor_b32 v12, 0x10000, v9\n" \
  "ds_read_b64_tr_b16 v[160:161], v12 offset:0\n" \
  "ds_read_b64_tr_b16 v[162:163], v12 offset:4096\n" \
  "ds_read_b64_tr_b16 v[164:165], v12 offset:8192\n" \
  "ds_read_b64_tr_b16 v[166:167], v12 offset:12288\n" \
  "v_xor_b32 v13, 0x10020, v9\n" \
  "ds_read_b64_tr_b16 v[224:225], v13 offset:0\n" \
  "ds_read_b64_tr_b16 v[226:227], v13 offset:4096\n" \
  "ds_read_b64_tr_b16 v[228:229], v13 offset:8192\n" \
  "ds_read_b64_tr_b16 v[230:231], v13 offset:12288\n" \
  "s_waitcnt lgkmcnt(0)\n" \
  "v_xor_b32 v12, 0x14000, v9\n" \
  "ds_read_b64_tr_b16 v[168:169], v12 offset:0\n" \
  "ds_read_b64_tr_b16 v[170:171], v12 offset:4096\n" \
  "ds_read_b64_tr_b16 v[172:173], v12 offset:8192\n" \
  "ds_read_b64_tr_b16 v[174:175], v12 offset:12288\n" \
  "v_xor_b32 v13, 0x14020, v9\n" \
  "ds_read_b64_tr_b16 v[232:233], v13 offset:0\n" \
  "ds_read_b64_tr_b16 v[234:235], v13 offset:4096\n" \
  "ds_read_b64_tr_b16 v[236:237], v13 offset:8192\n" \
  "ds_read_b64_tr_b16 v[238:239], v13 offset:12288\n" \
  "s_waitcnt lgkmcnt(0)\n" \
  "v_xor_b32 v12, 0x18000, v9\n" \
  "ds_read_b64_tr_b16 v[176:177], v12 offset:0\n" \
  "ds_read_b64_tr_b16 v[178:179], v12 offset:4096\n" \
  "ds_read_b64_tr_b16 v[180:181], v12 offset:8192\n" \
  "ds_read_b64_tr_b16 v[182:183], v12 offset:12288\n" \
  "v_xor_b32 v13, 0x18020, v9\n" \
  "ds_read_b64_tr_b16 v[240:241], v13 offset:0\n" \
  "ds_read_b64_tr_b16 v[242:243], v13 offset:4096\n" \
  "ds_read_b64_tr_b16 v[244:245], v13 offset:8192\n" \
  "ds_read_b64_tr_b16 v[246:247], v13 offset:12288\n" \
  "s_waitcnt lgkmcnt(0)\n" \
  "v_xor_b32 v12, 0x1c000, v9\n" \
  "ds_read_b64_tr_b16 v[184:185], v12 offset:0\n" \
  "ds_read_b64_tr_b16 v[186:187], v12 offset:4096\n" \
  "ds_read_b64_tr_b16 v[188:189], v12 offset:8192\n" \
  "ds_read_b64_tr_b16 v[190:191], v12 offset:12288\n" \
  "v_xor_b32 v13, 0x1c020, v9\n" \
  "ds_read_b64_tr_b16 v[248:249], v13 offset:0\n" \
  "ds_read_b64_tr_b16 v[250:251], v13 offset:4096\n" \
  "ds_read_b64_tr_b16 v[252:253], v13 offset:8192\n" \
  "ds_read_b64_tr_b16 v[254:255], v13 offset:12288\n" \
  "s_waitcnt lgkmcnt(0)\n" \
  "s_waitcnt lgkmcnt(0)\n" \
  "s_barrier\n" \
  "s_mov_b64 s[70:71], s[22:23]\n" \
  "s_add_u32 m0, s75, 0x10000\n" \
  "s_nop 0\n" \
  "global_load_lds_dwordx4 v1, s[70:71]\n" \
  "s_add_u32 m0, s75, 0x10400\n" \
  "s_nop 0\n" \
  "global_load_lds_dwordx4 v2, s[70:71]\n" \
  "s_add_u32 m0, s75, 0x10800\n" \
  "s_nop 0\n" \
  "global_load_lds_dwordx4 v3, s[70:71]\n" \
  "s_add_u32 m0, s75, 0x10c00\n" \
  "s_nop 0\n" \
  "global_load_lds_dwordx4 v4, s[70:71]\n" \
  "s_add_u32 s70, s70, 0x80000\n" \
  "s_addc_u32 s71, s71, 0\n" \
  "s_add_u32 m0, s75, 0x14000\n" \
  "s_nop 0\n" \
  "global_load_lds_dwordx4 v1, s[70:71]\n" \
  "s_add_u32 m0, s75, 0x14400\n" \
  "s_nop 0\n" \
  "global_load_lds_dwordx4 v2, s[70:71]\n" \
  "s_add_u32 m0, s75, 0x14800\n" \
  "s_nop 0\n" \
  "global_load_lds_dwordx4 v3, s[70:71]\n" \
  "s_add_u32 m0, s75, 0x14c00\n" \
  "s_nop 0\n" \
  "global_load_lds_dwordx4 v4, s[70:71]\n" \
  "s_add_u32 s70, s70, 0x80000\n" \
  "s_addc_u32 s71, s71, 0\n" \
  "s_add_u32 m0, s75, 0x18000\n" \
  "s_nop 0\n" \
  "global_load_lds_dwordx4 v1, s[70:71]\n" \
  "s_add_u32 m0, s75, 0x18400\n" \
  "s_nop 0\n" \
  "global_load_lds_dwordx4 v2, s[70:71]\n" \
  "s_add_u32 m0, s75, 0x18800\n" \
  "s_nop 0\n" \
  "global_load_lds_dwordx4 v3, s[70:71]\n" \
  "s_add_u32 m0, s75, 0x18c00\n" \
  "s_nop 0\n" \
  "global_load_lds_dwordx4 v4, s[70:71]\n" \
  "s_add_u32 s70, s70, 0x80000\n" \
  "s_addc_u32 s71, s71, 0\n" \
  "s_add_u32 m0, s75, 0x1c000\n" \
  "s_nop 0\n" \
  "global_load_lds_dwordx4 v1, s[70:71]\n" \
  "s_add_u32 m0, s75, 0x1c400\n" \
  "s_nop 0\n" \
  "global_load_lds_dwordx4 v2, s[70:71]\n" \
  "s_add_u32 m0, s75, 0x1c800\n" \
  "s_nop 0\n" \
  "global_load_lds_dwordx4 v3, s[70:71]\n" \
  "s_add_u32 m0, s75, 0x1cc00\n" \
  "s_nop 0\n" \
  "global_load_lds_dwordx4 v4, s[70:71]\n" \
  "s_waitcnt vmcnt(16)\n" \
  "s_barrier\n" \
  "v_xor_b32 v12, 0x0, v9\n" \
  "ds_read_b64_tr_b16 a[0:1], v12 offset:0\n" \
  "ds_read_b64_tr_b16 a[2:3], v12 offset:4096\n" \
  "ds_read_b64_tr_b16 a[4:5], v12 offset:8192\n" \
  "ds_read_b64_tr_b16 a[6:7], v12 offset:12288\n" \
  "v_xor_b32 v13, 0x20, v9\n" \
  "ds_read_b64_tr_b16 a[64:65], v13 offset:0\n" \
  "ds_read_b64_tr_b16 a[66:67], v13 offset:4096\n" \
  "ds_read_b64_tr_b16 a[68:69], v13 offset:8192\n" \
  "ds_read_b64_tr_b16 a[70:71], v13 offset:12288\n" \
  "s_waitcnt lgkmcnt(0)\n" \
  "v_xor_b32 v12, 0x4000, v9\n" \
  "ds_read_b64_tr_b16 a[8:9], v12 offset:0\n" \
  "ds_read_b64_tr_b16 a[10:11], v12 offset:4096\n" \
  "ds_read_b64_tr_b16 a[12:13], v12 offset:8192\n" \
  "ds_read_b64_tr_b16 a[14:15], v12 offset:12288\n" \
  "v_xor_b32 v13, 0x4020, v9\n" \
  "ds_read_b64_tr_b16 a[72:73], v13 offset:0\n" \
  "ds_read_b64_tr_b16 a[74:75], v13 offset:4096\n" \
  "ds_read_b64_tr_b16 a[76:77], v13 offset:8192\n" \
  "ds_read_b64_tr_b16 a[78:79], v13 offset:12288\n" \
  "s_waitcnt lgkmcnt(0)\n" \
  "v_xor_b32 v12, 0x8000, v9\n" \
  "ds_read_b64_tr_b16 a[16:17], v12 offset:0\n" \
  "ds_read_b64_tr_b16 a[18:19], v12 offset:4096\n" \
  "ds_read_b64_tr_b16 a[20:21], v12 offset:8192\n" \
  "ds_read_b64_tr_b16 a[22:23], v12 offset:12288\n" \
  "v_xor_b32 v13, 0x8020, v9\n" \
  "ds_read_b64_tr_b16 a[80:81], v13 offset:0\n" \
  "ds_read_b64_tr_b16 a[82:83], v13 offset:4096\n" \
  "ds_read_b64_tr_b16 a[84:85], v13 offset:8192\n" \
  "ds_read_b64_tr_b16 a[86:87], v13 offset:12288\n" \
  "s_waitcnt lgkmcnt(0)\n" \
  "v_xor_b32 v12, 0xc000, v9\n" \
  "ds_read_b64_tr_b16 a[24:25], v12 offset:0\n" \
  "ds_read_b64_tr_b16 a[26:27], v12 offset:4096\n" \
  "ds_read_b64_tr_b16 a[28:29], v12 offset:8192\n" \
  "ds_read_b64_tr_b16 a[30:31], v12 offset:12288\n" \
  "v_xor_b32 v13, 0xc020, v9\n" \
  "ds_read_b64_tr_b16 a[88:89], v13 offset:0\n" \
  "ds_read_b64_tr_b16 a[90:91], v13 offset:4096\n" \
  "ds_read_b64_tr_b16 a[92:93], v13 offset:8192\n" \
  "ds_read_b64_tr_b16 a[94:95], v13 offset:12288\n" \
  "s_waitcnt lgkmcnt(0)\n" \
  "s_waitcnt lgkmcnt(0)\n" \
  "s_barrier\n" \
  "s_mov_b64 s[70:71], s[24:25]\n" \
  "s_add_u32 m0, s96, 0\n" \
  "s_nop 0\n" \
  "global_load_lds_dwordx4 v5, s[70:71]\n" \
  "s_add_u32 m0, s96, 0x400\n" \
  "s_nop 0\n" \
  "global_load_lds_dwordx4 v6, s[70:71]\n" \
  "s_add_u32 s70, s70, 0x80000\n" \
  "s_addc_u32 s71, s71, 0\n" \
  "s_add_u32 m0, s96, 0x2000\n" \
  "s_nop 0\n" \
  "global_load_lds_dwordx4 v5, s[70:71]\n" \
  "s_add_u32 m0, s96, 0x2400\n" \
  "s_nop 0\n" \
  "global_load_lds_dwordx4 v6, s[70:71]\n" \
  "s_add_u32 s70, s70, 0x80000\n" \
  "s_addc_u32 s71, s71, 0\n" \
  "s_add_u32 m0, s96, 0x4000\n" \
  "s_nop 0\n" \
  "global_load_lds_dwordx4 v5, s[70:71]\n" \
  "s_add_u32 m0, s96, 0x4400\n" \
  "s_nop 0\n" \
  "global_load_lds_dwordx4 v6, s[70:71]\n" \
  "s_add_u32 s70, s70, 0x80000\n" \
  "s_addc_u32 s71, s71, 0\n" \
  "s_add_u32 m0, s96, 0x6000\n" \
  "s_nop 0\n" \
  "global_load_lds_dwordx4 v5, s[70:71]\n" \
  "s_add_u32 m0, s96, 0x6400\n" \
  "s_nop 0\n" \
  "global_load_lds_dwordx4 v6, s[70:71]\n" \
  "s_add_u32 s70, s70, 0x80000\n" \
  "s_addc_u32 s71, s71, 0\n" \
  "s_add_u32 m0, s96, 0x8000\n" \
  "s_nop 0\n" \
  "global_load_lds_dwordx4 v5, s[70:71]\n" \
  "s_add_u32 m0, s96, 0x8400\n" \
  "s_nop 0\n" \
  "global_load_lds_dwordx4 v6, s[70:71]\n" \
  "s_add_u32 s70, s70, 0x80000\n" \
  "s_addc_u32 s71, s71, 0\n" \
  "s_add_u32 m0, s96, 0xa000\n" \
  "s_nop 0\n" \
  "global_load_lds_dwordx4 v5, s[70:71]\n" \
  "s_add_u32 m0, s96, 0xa400\n" \
  "s_nop 0\n" \
  "global_load_lds_dwordx4 v6, s[70:71]\n" \
  "s_add_u32 s70, s70, 0x80000\n" \
  "s_addc_u32 s71, s71, 0\n" \
  "s_add_u32 m0, s96, 0xc000\n" \
  "s_nop 0\n" \
  "global_load_lds_dwordx4 v5, s[70:71]\n" \
  "s_add_u32 m0, s96, 0xc400\n" \
  "s_nop 0\n" \
  "global_load_lds_dwordx4 v6, s[70:71]\n" \
  "s_add_u32 s70, s70, 0x80000\n" \
  "s_addc_u32 s71, s71, 0\n" \
  "s_add_u32 m0, s96, 0xe000\n" \
  "s_nop 0\n" \
  "global_load_lds_dwordx4 v5, s[70:71]\n" \
  "s_add_u32 m0, s96, 0xe400\n" \
  "s_nop 0\n" \
  "global_load_lds_dwordx4 v6, s[70:71]\n" \
  "s_waitcnt vmcnt(16)\n" \
  "s_barrier\n" \
  "v_xor_b32 v12, 0x10000, v9\n" \
  "ds_read_b64_tr_b16 a[32:33], v12 offset:0\n" \
  "ds_read_b64_tr_b16 a[34:35], v12 offset:4096\n" \
  "ds_read_b64_tr_b16 a[36:37], v12 offset:8192\n" \
  "ds_read_b64_tr_b16 a[38:39], v12 offset:12288\n" \
  "v_xor_b32 v13, 0x10020, v9\n" \
  "ds_read_b64_tr_b16 a[96:97], v13 offset:0\n" \
  "ds_read_b64_tr_b16 a[98:99], v13 offset:4096\n" \
  "ds_read_b64_tr_b16 a[100:101], v13 offset:8192\n" \
  "ds_read_b64_tr_b16 a[102:103], v13 offset:12288\n" \
  "s_waitcnt lgkmcnt(0)\n" \
  "v_xor_b32 v12, 0x14000, v9\n" \
  "ds_read_b64_tr_b16 a[40:41], v12 offset:0\n" \
  "ds_read_b64_tr_b16 a[42:43], v12 offset:4096\n" \
  "ds_read_b64_tr_b16 a[44:45], v12 offset:8192\n" \
  "ds_read_b64_tr_b16 a[46:47], v12 offset:12288\n" \
  "v_xor_b32 v13, 0x14020, v9\n" \
  "ds_read_b64_tr_b16 a[104:105], v13 offset:0\n" \
  "ds_read_b64_tr_b16 a[106:107], v13 offset:4096\n" \
  "ds_read_b64_tr_b16 a[108:109], v13 offset:8192\n" \
  "ds_read_b64_tr_b16 a[110:111], v13 offset:12288\n" \
  "s_waitcnt lgkmcnt(0)\n" \
  "v_xor_b32 v12, 0x18000, v9\n" \
  "ds_read_b64_tr_b16 a[48:49], v12 offset:0\n" \
  "ds_read_b64_tr_b16 a[50:51], v12 offset:4096\n" \
  "ds_read_b64_tr_b16 a[52:53], v12 offset:8192\n" \
  "ds_read_b64_tr_b16 a[54:55], v12 offset:12288\n" \
  "v_xor_b32 v13, 0x18020, v9\n" \
  "ds_read_b64_tr_b16 a[112:113], v13 offset:0\n" \
  "ds_read_b64_tr_b16 a[114:115], v13 offset:4096\n" \
  "ds_read_b64_tr_b16 a[116:117], v13 offset:8192\n" \
  "ds_read_b64_tr_b16 a[118:119], v13 offset:12288\n" \
  "s_waitcnt lgkmcnt(0)\n" \
  "v_xor_b32 v12, 0x1c000, v9\n" \
  "ds_read_b64_tr_b16 a[56:57], v12 offset:0\n" \
  "ds_read_b64_tr_b16 a[58:59], v12 offset:4096\n" \
  "ds_read_b64_tr_b16 a[60:61], v12 offset:8192\n" \
  "ds_read_b64_tr_b16 a[62:63], v12 offset:12288\n" \
  "v_xor_b32 v13, 0x1c020, v9\n" \
  "ds_read_b64_tr_b16 a[120:121], v13 offset:0\n" \
  "ds_read_b64_tr_b16 a[122:123], v13 offset:4096\n" \
  "ds_read_b64_tr_b16 a[124:125], v13 offset:8192\n" \
  "ds_read_b64_tr_b16 a[126:127], v13 offset:12288\n" \
  "s_waitcnt lgkmcnt(0)\n" \
  "s_waitcnt lgkmcnt(0)\n" \
  "s_barrier\n" \
  "s_mov_b64 s[70:71], s[26:27]\n" \
  "s_add_u32 s70, s70, 0\n" \
  "s_addc_u32 s71, s71, 0\n" \
  "s_add_u32 m0, s73, 0x10000\n" \
  "s_nop 0\n" \
  "global_load_lds_dwordx4 v7, s[70:71]\n" \
  "s_mov_b64 s[70:71], s[26:27]\n" \
  "s_add_u32 s70, s70, 0x1000\n" \
  "s_addc_u32 s71, s71, 0\n" \
  "s_add_u32 m0, s73, 0x10400\n" \
  "s_nop 0\n" \
  "global_load_lds_dwordx4 v8, s[70:71]\n" \
  "s_mov_b64 s[70:71], s[26:27]\n" \
  "s_add_u32 s70, s70, 0x2000\n" \
  "s_addc_u32 s71, s71, 0\n" \
  "s_add_u32 m0, s73, 0x10800\n" \
  "s_nop 0\n" \
  "global_load_lds_dwordx4 v7, s[70:71]\n" \
  "s_mov_b64 s[70:71], s[26:27]\n" \
  "s_add_u32 s70, s70, 0x3000\n" \
  "s_addc_u32 s71, s71, 0\n" \
  "s_add_u32 m0, s73, 0x10c00\n" \
  "s_nop 0\n" \
  "global_load_lds_dwordx4 v8, s[70:71]\n" \
  "s_mov_b64 s[70:71], s[26:27]\n" \
  "s_add_u32 s70, s70, 0x4000\n" \
  "s_addc_u32 s71, s71, 0\n" \
  "s_add_u32 m0, s73, 0x11000\n" \
  "s_nop 0\n" \
  "global_load_lds_dwordx4 v7, s[70:71]\n" \
  "s_mov_b64 s[70:71], s[26:27]\n" \
  "s_add_u32 s70, s70, 0x5000\n" \
  "s_addc_u32 s71, s71, 0\n" \
  "s_add_u32 m0, s73, 0x11400\n" \
  "s_nop 0\n" \
  "global_load_lds_dwordx4 v8, s[70:71]\n" \
  "s_mov_b64 s[70:71], s[26:27]\n" \
  "s_add_u32 s70, s70, 0x6000\n" \
  "s_addc_u32 s71, s71, 0\n" \
  "s_add_u32 m0, s73, 0x11800\n" \
  "s_nop 0\n" \
  "global_load_lds_dwordx4 v7, s[70:71]\n" \
  "s_mov_b64 s[70:71], s[26:27]\n" \
  "s_add_u32 s70, s70, 0x7000\n" \
  "s_addc_u32 s71, s71, 0\n" \
  "s_add_u32 m0, s73, 0x11c00\n" \
  "s_nop 0\n" \
  "global_load_lds_dwordx4 v8, s[70:71]\n" \
  "s_mov_b64 s[70:71], s[26:27]\n" \
  "s_add_u32 s70, s70, 0x8000\n" \
  "s_addc_u32 s71, s71, 0\n" \
  "s_add_u32 m0, s73, 0x12000\n" \
  "s_nop 0\n" \
  "global_load_lds_dwordx4 v7, s[70:71]\n" \
  "s_mov_b64 s[70:71], s[26:27]\n" \
  "s_add_u32 s70, s70, 0x9000\n" \
  "s_addc_u32 s71, s71, 0\n" \
  "s_add_u32 m0, s73, 0x12400\n" \
  "s_nop 0\n" \
  "global_load_lds_dwordx4 v8, s[70:71]\n" \
  "s_mov_b64 s[70:71], s[26:27]\n" \
  "s_add_u32 s70, s70, 0xa000\n" \
  "s_addc_u32 s71, s71, 0\n" \
  "s_add_u32 m0, s73, 0x12800\n" \
  "s_nop 0\n" \
  "global_load_lds_dwordx4 v7, s[70:71]\n" \
  "s_mov_b64 s[70:71], s[26:27]\n" \
  "s_add_u32 s70, s70, 0xb000\n" \
  "s_addc_u32 s71, s71, 0\n" \
  "s_add_u32 m0, s73, 0x12c00\n" \
  "s_nop 0\n" \
  "global_load_lds_dwordx4 v8, s[70:71]\n" \
  "s_mov_b64 s[70:71], s[26:27]\n" \
  "s_add_u32 s70, s70, 0xc000\n" \
  "s_addc_u32 s71, s71, 0\n" \
  "s_add_u32 m0, s73, 0x13000\n" \
  "s_nop 0\n" \
  "global_load_lds_dwordx4 v7, s[70:71]\n" \
  "s_mov_b64 s[70:71], s[26:27]\n" \
  "s_add_u32 s70, s70, 0xd000\n" \
  "s_addc_u32 s71, s71, 0\n" \
  "s_add_u32 m0, s73, 0x13400\n" \
  "s_nop 0\n" \
  "global_load_lds_dwordx4 v8, s[70:71]\n" \
  "s_mov_b64 s[70:71], s[26:27]\n" \
  "s_add_u32 s70, s70, 0xe000\n" \
  "s_addc_u32 s71, s71, 0\n" \
  "s_add_u32 m0, s73, 0x13800\n" \
  "s_nop 0\n" \
  "global_load_lds_dwordx4 v7, s[70:71]\n" \
  "s_mov_b64 s[70:71], s[26:27]\n" \
  "s_add_u32 s70, s70, 0xf000\n" \
  "s_addc_u32 s71, s71, 0\n" \
  "s_add_u32 m0, s73, 0x13c00\n" \
  "s_nop 0\n" \
  "global_load_lds_dwordx4 v8, s[70:71]\n" \
  "s_waitcnt vmcnt(16)\n" \
  "s_barrier\n" \
  "v_xor_b32 v12, 0x0, v10\n" \
  "ds_read_b64_tr_b16 a[128:129], v12 offset:0\n" \
  "ds_read_b64_tr_b16 a[130:131], v12 offset:2048\n" \
  "ds_read_b64_tr_b16 a[132:133], v12 offset:4096\n" \
  "ds_read_b64_tr_b16 a[134:135], v12 offset:6144\n" \
  "v_xor_b32 v13, 0x2000, v10\n" \
  "ds_read_b64_tr_b16 a[136:137], v13 offset:0\n" \
  "ds_read_b64_tr_b16 a[138:139], v13 offset:2048\n" \
  "ds_read_b64_tr_b16 a[140:141], v13 offset:4096\n" \
  "ds_read_b64_tr_b16 a[142:143], v13 offset:6144\n" \
  "s_waitcnt lgkmcnt(0)\n" \
  "v_xor_b32 v12, 0x4000, v10\n" \
  "ds_read_b64_tr_b16 a[144:145], v12 offset:0\n" \
  "ds_read_b64_tr_b16 a[146:147], v12 offset:2048\n" \
  "ds_read_b64_tr_b16 a[148:149], v12 offset:4096\n" \
  "ds_read_b64_tr_b16 a[150:151], v12 offset:6144\n" \
  "v_xor_b32 v13, 0x6000, v10\n" \
  "ds_read_b64_tr_b16 a[152:153], v13 offset:0\n" \
  "ds_read_b64_tr_b16 a[154:155], v13 offset:2048\n" \
  "ds_read_b64_tr_b16 a[156:157], v13 offset:4096\n" \
  "ds_read_b64_tr_b16 a[158:159], v13 offset:6144\n" \
  "s_waitcnt lgkmcnt(0)\n" \
  "v_xor_b32 v12, 0x8000, v10\n" \
  "ds_read_b64_tr_b16 a[160:161], v12 offset:0\n" \
  "ds_read_b64_tr_b16 a[162:163], v12 offset:2048\n" \
  "ds_read_b64_tr_b16 a[164:165], v12 offset:4096\n" \
  "ds_read_b64_tr_b16 a[166:167], v12 offset:6144\n" \
  "v_xor_b32 v13, 0xa000, v10\n" \
  "ds_read_b64_tr_b16 a[168:169], v13 offset:0\n" \
  "ds_read_b64_tr_b16 a[170:171], v13 offset:2048\n" \
  "ds_read_b64_tr_b16 a[172:173], v13 offset:4096\n" \
  "ds_read_b64_tr_b16 a[174:175], v13 offset:6144\n" \
  "s_waitcnt lgkmcnt(0)\n" \
  "v_xor_b32 v12, 0xc000, v10\n" \
  "ds_read_b64_tr_b16 a[176:177], v12 offset:0\n" \
  "ds_read_b64_tr_b16 a[178:179], v12 offset:2048\n" \
  "ds_read_b64_tr_b16 a[180:181], v12 offset:4096\n" \
  "ds_read_b64_tr_b16 a[182:183], v12 offset:6144\n" \
  "v_xor_b32 v13, 0xe000, v10\n" \
  "ds_read_b64_tr_b16 a[184:185], v13 offset:0\n" \
  "ds_read_b64_tr_b16 a[186:187], v13 offset:2048\n" \
  "ds_read_b64_tr_b16 a[188:189], v13 offset:4096\n" \
  "ds_read_b64_tr_b16 a[190:191], v13 offset:6144\n" \
  "s_waitcnt lgkmcnt(0)\n" \
  "s_waitcnt lgkmcnt(0)\n" \
  "s_barrier\n" \
  "s_mov_b32 s26, 0x7f817f81\n" \
  "s_mov_b32 s27, 0\n" \
  "s_mov_b32 s12, 0x3f803f80\n" \
  "s_mov_b32 s13, 0x3f803f80\n" \
  "s_cmp_eq_u32 s54, 0\n" \
  "s_cselect_b64 s[10:11], 1, 0\n" \
  "v_lshlrev_b32 v21, 3, v113\n" \
  "s_mov_b32 s69, 0\n" \
  "v_readfirstlane_b32 s64, v22\n" \
  "s_nop 0\n" \
  "s_add_u32 s64, s64, 0x9e3779b9\n" \
  "s_add_u32 s65, s30, 0x2000\n" \
  "L_inip_%=:\n" \
  "s_nop 7\n" \
  "buffer_load_dword v22, v21, s[36:39], s65 offen sc1\n" \
  "buffer_load_dwordx2 v[24:25], v20, s[36:39], s30 offen offset:256 sc1\n" \
  "s_waitcnt vmcnt(0)\n" \
  "v_cmp_ne_u32 vcc, s64, v22\n" \
  "v_cmp_ne_u32 s[70:71], s64, v25\n" \
  "s_nop 0\n" \
  "s_or_b64 vcc, vcc, s[70:71]\n" \
  "s_cbranch_scc0 L_inid_%=\n" \
  "s_add_u32 s69, s69, 1\n" \
  "s_cmp_gt_u32 s69, 0x80000\n" \
  "s_cbranch_scc1 L_inid_%=\n" \
  "s_sleep 2\n" \
  "s_branch L_inip_%=\n" \
  "L_inid_%=:\n" \
  "s_nop 7\n" \
  "v_and_b32 v21, 16, v113\n" \
  "v_lshlrev_b32 v21, 3, v21\n" \
  "buffer_load_dword v22, v21, s[36:39], s5 offen sc1\n" \
  "v_readfirstlane_b32 s31, v24\n" \
  "s_nop 0\n" \
  "s_and_b32 s30, s31, 1\n" \
  "s_lshl_b32 s65, s31, 5\n" \
  "s_sub_u32 s1, s1, s65\n" \
  "s_mov_b32 s29, 0\n" \
  "s_xor_b32 s65, s30, 0\n" \
  "s_lshl_b32 s65, s65, 13\n" \
  "s_add_u32 s56, s65, 0\n" \
  "s_xor_b32 s65, s30, 1\n" \
  "s_lshl_b32 s65, s65, 13\n" \
  "s_add_u32 s48, s65, 0x200000\n" \
  "s_xor_b32 s65, s30, 0\n" \
  "s_lshl_b32 s65, s65, 13\n" \
  "s_add_u32 s49, s65, 0x400000\n" \
  "s_xor_b32 s65, s30, 1\n" \
  "s_lshl_b32 s65, s65, 13\n" \
  "s_add_u32 s50, s65, 0x600000\n" \
  "s_mov_b32 s33, 0\n" \
  "s_mov_b32 s72, 0\n" \
  "s_mov_b32 s69, 0\n" \
  "s_add_u32 s28, s28, s56\n" \
  "s_mov_b32 s41, 0x4000\n" \
  "s_mov_b32 s42, 0x7000\n" \
  "s_mov_b32 s43, 0xa000\n" \
  "s_sub_u32 s59, s54, 1\n" \
  "s_mul_i32 s67, s59, 0xa00\n" \
  "s_add_u32 s67, s67, s55\n" \
  "s_lshl_b32 s59, s59, 12\n" \
  "s_add_u32 s68, s53, 0xe000\n" \
  "s_mov_b32 s7, 0\n" \
  "s_mov_b32 s14, 0\n" \
  "s_and_b32 s19, s7, 1\n" \
  "s_lshl_b32 s19, s19, 7\n" \
  "s_add_u32 s19, s19, s5\n" \
  "s_lshr_b32 s6, s7, 1\n" \
  "s_add_u32 s6, s6, 1\n" \
  "s_min_u32 s6, s6, 32\n" \
  "s_lshr_b32 s32, s33, 3\n" \
  "s_lshl_b32 s32, s32, 18\n" \
  "s_add_u32 s32, s32, s56\n" \
  "s_and_b32 s74, s33, 7\n" \
  "s_add_u32 s74, s74, 1\n" \
  "s_lshl_b32 s74, s74, 14\n" \
  "s_add_u32 s34, s32, s74\n" \
  "s_lshl_b32 s35, s54, 10\n" \
  "s_add_u32 s35, s35, s34\n" \
  "s_add_u32 s40, s35, 0x1000\n" \
  "s_and_b32 s74, s33, 3\n" \
  "s_lshl_b32 s74, s74, 11\n" \
  "s_add_u32 s32, s32, s74\n" \
  "s_and_b32 s74, s33, 4\n" \
  "s_lshl_b32 s74, s74, 12\n" \
  "s_add_u32 s32, s32, s74\n" \
  "s_add_u32 s32, s32, 0x24000\n" \
  "s_add_u32 s44, s48, 0x40000\n" \
  "s_mov_b32 m0, s68\n" \
  "s_nop 0\n" \
  "buffer_load_dwordx4 v113, s[36:39], s35 offen sc1 lds\n" \
  "s_xor_b32 m0, m0, 0x1000\n" \
  "s_nop 0\n" \
  "buffer_load_dwordx4 v113, s[36:39], s40 offen sc1 lds\n" \
  "v_xor_b32 v12, 0, v11\n" \
  "ds_read_b64_tr_b16 a[192:193], v12 offset:0\n" \
  "ds_read_b64_tr_b16 a[194:195], v12 offset:512\n" \
  "ds_read_b64_tr_b16 a[196:197], v12 offset:4096\n" \
  "ds_read_b64_tr_b16 a[198:199], v12 offset:4608\n" \
  "ds_read_b64_tr_b16 a[200:201], v12 offset:8192\n" \
  "ds_read_b64_tr_b16 a[202:203], v12 offset:8704\n" \
  "ds_read_b64_tr_b16 a[204:205], v12 offset:12288\n" \
  "ds_read_b64_tr_b16 a[206:207], v12 offset:12800\n" \
  "s_waitcnt lgkmcnt(0)\n" \
  "v_xor_b32 v12, 64, v11\n" \
  "ds_read_b64_tr_b16 a[208:209], v12 offset:0\n" \
  "ds_read_b64_tr_b16 a[210:211], v12 offset:512\n" \
  "ds_read_b64_tr_b16 a[212:213], v12 offset:4096\n" \
  "ds_read_b64_tr_b16 a[214:215], v12 offset:4608\n" \
  "ds_read_b64_tr_b16 a[216:217], v12 offset:8192\n" \
  "ds_read_b64_tr_b16 a[218:219], v12 offset:8704\n" \
  "ds_read_b64_tr_b16 a[220:221], v12 offset:12288\n" \
  "ds_read_b64_tr_b16 a[222:223], v12 offset:12800\n" \
  "s_waitcnt lgkmcnt(0)\n" \
  "v_xor_b32 v12, 128, v11\n" \
  "ds_read_b64_tr_b16 a[224:225], v12 offset:0\n" \
  "ds_read_b64_tr_b16 a[226:227], v12 offset:512\n" \
  "ds_read_b64_tr_b16 a[228:229], v12 offset:4096\n" \
  "ds_read_b64_tr_b16 a[230:231], v12 offset:4608\n" \
  "ds_read_b64_tr_b16 a[232:233], v12 offset:8192\n" \
  "ds_read_b64_tr_b16 a[234:235], v12 offset:8704\n" \
  "ds_read_b64_tr_b16 a[236:237], v12 offset:12288\n" \
  "ds_read_b64_tr_b16 a[238:239], v12 offset:12800\n" \
  "s_waitcnt lgkmcnt(0)\n" \
  "v_xor_b32 v12, 192, v11\n" \
  "ds_read_b64_tr_b16 a[240:241], v12 offset:0\n" \
  "ds_read_b64_tr_b16 a[242:243], v12 offset:512\n" \
  "ds_read_b64_tr_b16 a[244:245], v12 offset:4096\n" \
  "ds_read_b64_tr_b16 a[246:247], v12 offset:4608\n" \
  "ds_read_b64_tr_b16 a[248:249], v12 offset:8192\n" \
  "ds_read_b64_tr_b16 a[250:251], v12 offset:8704\n" \
  "ds_read_b64_tr_b16 a[252:253], v12 offset:12288\n" \
  "ds_read_b64_tr_b16 a[254:255], v12 offset:12800\n" \
  "s_waitcnt lgkmcnt(0)\n" \
  "s_mov_b32 s4, s16\n" \
  "s_mov_b32 s71, 0\n" \
  "s_cmp_eq_u32 s54, 0\n" \
  "s_cbranch_scc1 L_kv0_%=\n" \
  "s_waitcnt vmcnt(0)\n" \
  "s_branch L_k01c_%=\n" \
  "L_k01_%=:\n" \
  "s_nop 7\n" \
  "buffer_load_dword v22, v21, s[36:39], s5 offen sc1\n" \
  "s_waitcnt vmcnt(0)\n" \
  "L_k01c_%=:\n" \
  "s_nop 7\n" \
  "v_cmp_eq_u32 vcc, 0, v22\n" \
  "s_cbranch_vccz L_k01d_%=\n" \
  "s_add_u32 s71, s71, 1\n" \
  "s_cmp_gt_u32 s71, 0x80000\n" \
  "s_cbranch_scc1 L_k01d_%=\n" \
  "s_sleep 1\n" \
  "s_branch L_k01_%=\n" \
  "L_k01d_%=:\n" \
  "s_nop 7\n" \
  "s_add_u32 m0, s41, s59\n" \
  "s_lshr_b32 s74, s51, 21\n" \
  "s_xor_b32 s74, s74, s30\n" \
  "s_and_b32 s74, s74, 1\n" \
  "s_lshl_b32 s74, s74, 13\n" \
  "s_add_u32 s74, s74, s51\n" \
  "buffer_load_dwordx4 v113, s[36:39], s74 offen sc1 lds\n" \
  "s_add_u32 m0, m0, 0x400\n" \
  "s_add_u32 s74, s74, 0x400\n" \
  "buffer_load_dwordx4 v113, s[36:39], s74 offen sc1 lds\n" \
  "s_add_u32 m0, m0, 0x400\n" \
  "s_add_u32 s74, s74, 0x400\n" \
  "buffer_load_dwordx4 v113, s[36:39], s74 offen sc1 lds\n" \
  "s_add_u32 m0, m0, 0x400\n" \
  "s_add_u32 s74, s74, 0x400\n" \
  "buffer_load_dwordx4 v113, s[36:39], s74 offen sc1 lds\n" \
  "v_mov_b32 v0, 0\n" \
  "s_add_u32 s7, s7, 1\n" \
  "s_and_b32 s19, s7, 1\n" \
  "s_lshl_b32 s19, s19, 7\n" \
  "s_add_u32 s19, s19, s5\n" \
  "s_lshr_b32 s6, s7, 1\n" \
  "s_add_u32 s6, s6, 1\n" \
  "s_min_u32 s6, s6, 32\n" \
  "s_lshl_b32 m0, s54, 8\n" \
  "s_add_u32 m0, m0, 0x3400\n" \
  "s_nop 0\n" \
  "buffer_load_dword v0, s[36:39], s19 offen sc1 lds\n" \
  "s_cmp_lt_u32 s7, 64\n" \
  "s_cselect_b32 s74, 0x80000, 0\n" \
  "s_add_u32 s51, s51, s74\n" \
  "s_add_u32 m0, s42, s59\n" \
  "s_lshr_b32 s74, s51, 21\n" \
  "s_xor_b32 s74, s74, s30\n" \
  "s_and_b32 s74, s74, 1\n" \
  "s_lshl_b32 s74, s74, 13\n" \
  "s_add_u32 s74, s74, s51\n" \
  "buffer_load_dwordx4 v113, s[36:39], s74 offen sc1 lds\n" \
  "s_add_u32 m0, m0, 0x400\n" \
  "s_add_u32 s74, s74, 0x400\n" \
  "buffer_load_dwordx4 v113, s[36:39], s74 offen sc1 lds\n" \
  "s_add_u32 m0, m0, 0x400\n" \
  "s_add_u32 s74, s74, 0x400\n" \
  "buffer_load_dwordx4 v113, s[36:39], s74 offen sc1 lds\n" \
  "s_add_u32 m0, m0, 0x400\n" \
  "s_add_u32 s74, s74, 0x400\n" \
  "buffer_load_dwordx4 v113, s[36:39], s74 offen sc1 lds\n" \
  "v_mov_b32 v0, 0\n" \
  "s_add_u32 s7, s7, 1\n" \
  "s_and_b32 s19, s7, 1\n" \
  "s_lshl_b32 s19, s19, 7\n" \
  "s_add_u32 s19, s19, s5\n" \
  "s_lshr_b32 s6, s7, 1\n" \
  "s_add_u32 s6, s6, 1\n" \
  "s_min_u32 s6, s6, 32\n" \
  "s_lshl_b32 m0, s54, 8\n" \
  "s_add_u32 m0, m0, 0x3400\n" \
  "s_nop 0\n" \
  "buffer_load_dword v0, s[36:39], s19 offen sc1 lds\n" \
  "s_cmp_lt_u32 s7, 64\n" \
  "s_cselect_b32 s74, 0x80000, 0\n" \
  "s_add_u32 s51, s51, s74\n" \
  "v_mov_b32 v8, s26\n" \
  "v_mov_b32 v9, s26\n" \
  "v_mov_b32 v10, s26\n" \
  "v_mov_b32 v11, s26\n" \
  "s_add_u32 s47, s49, s67\n" \
  "buffer_store_dwordx4 v[8:11], v113, s[36:39], s47 offen offset:0 sc1\n" \
  "buffer_store_dwordx4 v[8:11], v113, s[36:39], s47 offen offset:1024 sc1\n" \
  "buffer_store_dwordx4 v[8:11], v113, s[36:39], s47 offen offset:2048 sc1\n" \
  "L_kv0_%=:\n" \
  "s_nop 7\n" \
  "s_memtime s[64:65]\n" \
  "s_nop 0\n" \
  "s_memrealtime s[20:21]\n" \
  "s_nop 0\n" \
  "s_memrealtime s[22:23]\n" \
  "s_nop 0\n" \
  "s_memrealtime s[24:25]\n" \
  "s_nop 0\n" \
  "s_memrealtime s[76:77]\n" \
  "s_nop 0\n" \
  "s_memrealtime s[78:79]\n" \
  "s_nop 0\n" \
  "s_memrealtime s[80:81]\n" \
  "s_nop 0\n" \
  "s_memrealtime s[82:83]\n" \
  "s_nop 0\n" \
  "s_memrealtime s[84:85]\n" \
  "s_nop 0\n" \
  "s_memrealtime s[86:87]\n" \
  "s_nop 0\n" \
  "s_memrealtime s[88:89]\n" \
  "s_nop 0\n" \
  "s_memrealtime s[90:91]\n" \
  "s_nop 0\n" \
  "s_memrealtime s[92:93]\n" \
  "s_nop 0\n" \
  "s_memrealtime s[94:95]\n" \
  "s_mov_b32 s66, 0\n" \
  "s_waitcnt lgkmcnt(0)\n" \
  "s_cmp_eq_u32 s22, s20\n" \
  "s_addc_u32 s66, s66, 0\n" \
  "s_cmp_eq_u32 s24, s20\n" \
  "s_addc_u32 s66, s66, 0\n" \
  "s_cmp_eq_u32 s76, s20\n" \
  "s_addc_u32 s66, s66, 0\n" \
  "s_cmp_eq_u32 s78, s20\n" \
  "s_addc_u32 s66, s66, 0\n" \
  "s_cmp_eq_u32 s80, s20\n" \
  "s_addc_u32 s66, s66, 0\n" \
  "s_cmp_eq_u32 s82, s20\n" \
  "s_addc_u32 s66, s66, 0\n" \
  "s_cmp_eq_u32 s84, s20\n" \
  "s_addc_u32 s66, s66, 0\n" \
  "s_cmp_eq_u32 s86, s20\n" \
  "s_addc_u32 s66, s66, 0\n" \
  "s_cmp_eq_u32 s88, s20\n" \
  "s_addc_u32 s66, s66, 0\n" \
  "s_cmp_eq_u32 s90, s20\n" \
  "s_addc_u32 s66, s66, 0\n" \
  "s_cmp_eq_u32 s92, s20\n" \
  "s_addc_u32 s66, s66, 0\n" \
  "s_cmp_eq_u32 s94, s20\n" \
  "s_addc_u32 s66, s66, 0\n" \
  "s_lshl_b32 s66, s66, 3\n" \
  "s_add_u32 s64, s64, s66\n" \
  "s_sub_u32 s65, s20, s31\n" \
  "s_add_u32 s65, s65, 4\n" \
  "s_lshl_b32 s65, s65, 5\n" \
  "s_sub_u32 s66, s64, s0\n" \
  "s_sub_u32 s70, s65, s1\n" \
  "v_cvt_f32_u32 v24, s66\n" \
  "v_cvt_f32_u32 v25, s70\n" \
  "v_rcp_f32 v26, v24\n" \
  "v_rcp_f32 v27, v25\n" \
  "v_mul_f32 v24, 0x45800000, v24\n" \
  "v_mul_f32 v25, 0x45800000, v25\n" \
  "v_fma_f32 v26, v26, v25, 0.5\n" \
  "v_fma_f32 v27, v27, v24, 0.5\n" \
  "v_cvt_u32_f32 v26, v26\n" \
  "v_cvt_u32_f32 v27, v27\n" \
  "v_readfirstlane_b32 s66, v26\n" \
  "v_readfirstlane_b32 s70, v27\n" \
  "s_max_u32 s66, s66, 0x1324\n" \
  "s_min_u32 s66, s66, 0x2af8\n" \
  "s_max_u32 s70, s70, 0x5f5\n" \
  "s_min_u32 s70, s70, 0xd60\n" \
  "s_mov_b32 s2, s66\n" \
  "s_mov_b32 s3, s70\n" \
  "s_mov_b32 s0, s64\n" \
  "s_mov_b32 s1, s65\n" \
  "v_mov_b32 v24, s0\n" \
  "v_mov_b32 v25, s1\n" \
  "v_mov_b32 v26, s2\n" \
  "v_mov_b32 v27, s3\n" \
  "v_mov_b32 v28, 0x2c00\n" \
  "ds_write_b128 v28, v[24:27]\n" \
  "s_waitcnt vmcnt(0)\n" \
  "s_waitcnt lgkmcnt(0)\n" \
  "s_barrier\n" \
  "v_add_u32 v104, s41, v113\n" \
  "v_add_u32 v105, s41, v124\n" \
  "ds_read_b128 v[48:51], v104 offset:0\n" \
  "ds_read_b128 v[52:55], v104 offset:1024\n" \
  "ds_read_b128 v[56:59], v104 offset:2048\n" \
  "ds_read_b128 v[60:63], v104 offset:3072\n" \
  "ds_read_b128 v[64:67], v104 offset:4096\n" \
  "ds_read_b128 v[68:71], v104 offset:5120\n" \
  "ds_read_b128 v[72:75], v104 offset:6144\n" \
  "ds_read_b128 v[76:79], v104 offset:7168\n" \
  "ds_read_b64 v[80:81], v105\n" \
  "ds_read_b64 v[108:109], v105 offset:8\n" \
  "s_mov_b32 s74, s41\n" \
  "s_mov_b32 s41, s42\n" \
  "s_mov_b32 s42, s43\n" \
  "s_mov_b32 s43, s74\n" \
  "v_mov_b64 v[88:89], 0\n" \
  "v_mov_b64 v[90:91], 0\n" \
  "v_mov_b64 v[92:93], 0\n" \
  "v_mov_b64 v[94:95], 0\n" \
  "v_mov_b64 v[96:97], 0\n" \
  "v_mov_b64 v[98:99], 0\n" \
  "v_mov_b64 v[100:101], 0\n" \
  "v_mov_b64 v[102:103], 0\n" \
  "v_mov_b32 v4, 0x2c00\n" \
  "ds_read_b128 v[0:3], v4\n" \
  "v_mov_b64 v[84:85], 0\n" \
  "v_mov_b64 v[86:87], 0\n" \
  "s_add_u32 s45, s48, s55\n" \
  "s_add_u32 s46, s45, 0x1000\n" \
  "s_waitcnt lgkmcnt(0)\n" \
  "v_readfirstlane_b32 s0, v0\n" \
  "v_readfirstlane_b32 s1, v1\n" \
  "v_readfirstlane_b32 s2, v2\n" \
  "v_readfirstlane_b32 s3, v3\n" \
  "s_sub_u32 s53, s15, s0\n" \
  "v_xor_b32 v37, 0x400, v116\n" \
  "v_xor_b32 v38, 0x800, v116\n" \
  "v_xor_b32 v39, 0xc00, v116\n" \
  "v_mov_b32 v105, s16\n" \
  "v_mov_b32 v106, s18\n" \
  "v_bfrev_b32 v107, 2\n" \
  "v_cndmask_b32 v106, v107, v106, s[10:11]\n" \
  "s_waitcnt vmcnt(0)\n" \
  "s_branch L_body_%=\n" \
  ".p2align 6\n" \
  "L_body_%=:\n" \
  "ds_read_b128 v[0:3], v116 offset:57344\n" \
  "ds_read_b128 v[16:19], v116 offset:61440\n" \
  "s_barrier\n" \
  "ds_read_b128 v[4:7], v37 offset:57344\n" \
  "s_waitcnt lgkmcnt(2)\n" \
  "v_smfmac_f32_16x16x64_bf16 v[84:87], v[0:3], v[128:135], v112\n" \
  "ds_read_b128 v[20:23], v37 offset:61440\n" \
  "v_smfmac_f32_16x16x64_bf16 v[88:91], v[0:3], v[192:199], v112\n" \
  "s_waitcnt lgkmcnt(2)\n" \
  "s_memtime s[64:65]\n" \
  "v_smfmac_f32_16x16x64_bf16 v[84:87], v[16:19], v[160:167], v112\n" \
  "ds_read_b128 v[8:11], v38 offset:57344\n" \
  "v_smfmac_f32_16x16x64_bf16 v[88:91], v[16:19], v[224:231], v112\n" \
  "s_waitcnt lgkmcnt(2)\n" \
  "v_smfmac_f32_16x16x64_bf16 v[84:87], v[4:7], v[136:143], v112\n" \
  "ds_read_b128 v[24:27], v38 offset:61440\n" \
  "s_add_u32 s66, s64, s53\n" \
  "s_mul_i32 s66, s66, s2\n" \
  "v_smfmac_f32_16x16x64_bf16 v[88:91], v[4:7], v[200:207], v112\n" \
  "s_ashr_i32 s66, s66, 12\n" \
  "s_add_u32 s66, s66, s1\n" \
  "s_waitcnt lgkmcnt(2)\n" \
  "v_smfmac_f32_16x16x64_bf16 v[84:87], v[20:23], v[168:175], v112\n" \
  "ds_read_b128 v[12:15], v39 offset:57344\n" \
  "v_mov_b32 v104, s66\n" \
  "v_smfmac_f32_16x16x64_bf16 v[88:91], v[20:23], v[232:239], v112\n" \
  "buffer_store_dwordx2 v[104:105], v106, s[36:39], s32 offen sc1\n" \
  "s_waitcnt lgkmcnt(2)\n" \
  "v_smfmac_f32_16x16x64_bf16 v[84:87], v[8:11], v[144:151], v112\n" \
  "ds_read_b128 v[28:31], v39 offset:61440\n" \
  "v_smfmac_f32_16x16x64_bf16 v[88:91], v[8:11], v[208:215], v112\n" \
  "s_waitcnt lgkmcnt(2)\n" \
  "v_smfmac_f32_16x16x64_bf16 v[84:87], v[24:27], v[176:183], v112\n" \
  "v_smfmac_f32_16x16x64_bf16 v[88:91], v[24:27], v[240:247], v112\n" \
  "s_waitcnt lgkmcnt(1)\n" \
  "v_smfmac_f32_16x16x64_bf16 v[84:87], v[12:15], v[152:159], v112\n" \
  "v_smfmac_f32_16x16x64_bf16 v[88:91], v[12:15], v[216:223], v112\n" \
  "s_waitcnt lgkmcnt(0)\n" \
  "v_smfmac_f32_16x16x64_bf16 v[84:87], v[28:31], v[184:191], v112\n" \
  "s_mov_b32 s17, s64\n" \
  "v_smfmac_f32_16x16x64_bf16 v[88:91], v[28:31], v[248:255], v112\n" \
  "s_nop 1\n" \
  "v_smfmac_f32_16x16x64_bf16 v[92:95], v[0:3], a[0:7], v112\n" \
  "s_nop 1\n" \
  "v_smfmac_f32_16x16x64_bf16 v[92:95], v[4:7], a[8:15], v112\n" \
  "v_add_f32 v84, v84, v86\n" \
  "v_smfmac_f32_16x16x64_bf16 v[92:95], v[8:11], a[16:23], v112\n" \
  "v_add_f32 v85, v85, v87\n" \
  "v_smfmac_f32_16x16x64_bf16 v[92:95], v[12:15], a[24:31], v112\n" \
  "v_add_f32 v88, v88, v90\n" \
  "v_smfmac_f32_16x16x64_bf16 v[92:95], v[16:19], a[32:39], v112\n" \
  "v_add_f32 v89, v89, v91\n" \
  "v_smfmac_f32_16x16x64_bf16 v[92:95], v[20:23], a[40:47], v112\n" \
  "v_cvt_pk_bf16_f32 v32, v84, v85\n" \
  "v_smfmac_f32_16x16x64_bf16 v[96:99], v[0:3], a[64:71], v112\n" \
  "v_cvt_pk_bf16_f32 v88, v88, v89\n" \
  "ds_write2_b32 v119, v32, v88 offset1:64\n" \
  "v_smfmac_f32_16x16x64_bf16 v[92:95], v[24:27], a[48:55], v112\n" \
  "v_cmp_u_f32 vcc, v84, v85\n" \
  "v_smfmac_f32_16x16x64_bf16 v[96:99], v[4:7], a[72:79], v112\n" \
  "v_smfmac_f32_16x16x64_bf16 v[92:95], v[28:31], a[56:63], v112\n" \
  "v_smfmac_f32_16x16x64_bf16 v[96:99], v[8:11], a[80:87], v112\n" \
  "s_cbranch_vccnz L_gchk_%=\n" \
  "L_conta_%=:\n" \
  "s_waitcnt lgkmcnt(0)\n" \
  "s_barrier\n" \
  "v_smfmac_f32_16x16x64_bf16 v[96:99], v[12:15], a[88:95], v112\n" \
  "ds_read_b64_tr_b16 v[32:33], v121 offset:0\n" \
  "ds_read_b64_tr_b16 v[34:35], v121 offset:256\n" \
  "v_smfmac_f32_16x16x64_bf16 v[96:99], v[16:19], a[96:103], v112\n" \
  "ds_read_b64_tr_b16 v[36:37], v121 offset:512\n" \
  "ds_read_b64_tr_b16 v[38:39], v121 offset:768\n" \
  "v_smfmac_f32_16x16x64_bf16 v[96:99], v[20:23], a[104:111], v112\n" \
  "ds_read_b64_tr_b16 v[40:41], v121 offset:1024\n" \
  "ds_read_b64_tr_b16 v[42:43], v121 offset:1280\n" \
  "v_add_f32 v92, v92, v94\n" \
  "v_smfmac_f32_16x16x64_bf16 v[96:99], v[24:27], a[112:119], v112\n" \
  "ds_read_b64_tr_b16 v[44:45], v121 offset:1536\n" \
  "v_add_f32 v93, v93, v95\n" \
  "v_smfmac_f32_16x16x64_bf16 v[96:99], v[28:31], a[120:127], v112\n" \
  "ds_read_b64_tr_b16 v[46:47], v121 offset:1792\n" \
  "v_cvt_pk_bf16_f32 v92, v92, v93\n" \
  "v_smfmac_f32_16x16x64_bf16 v[100:103], v[0:3], a[128:135], v112\n" \
  "s_add_u32 s0, s0, s58\n" \
  "s_cmp_eq_u32 s33, 63\n" \
  "v_mov_b32 v95, 0\n" \
  "v_smfmac_f32_16x16x64_bf16 v[100:103], v[4:7], a[136:143], v112\n" \
  "s_nop 0\n" \
  "v_add_f32 v96, v96, v98\n" \
  "v_add_f32 v97, v97, v99\n" \
  "v_cvt_pk_bf16_f32 v96, v96, v97\n" \
  "ds_write2_b32 v120, v92, v96 offset1:128\n" \
  "ds_write2_b32 v115, v92, v96 offset1:128\n" \
  "s_waitcnt lgkmcnt(2)\n" \
  "v_mfma_f32_16x16x32_bf16 v[84:87], v[48:51], v[32:35], 0\n" \
  "v_mfma_f32_16x16x32_bf16 v[88:91], v[64:67], v[32:35], 0\n" \
  "v_mfma_f32_16x16x32_bf16 v[84:87], v[52:55], v[36:39], v[84:87]\n" \
  "v_mfma_f32_16x16x32_bf16 v[88:91], v[68:71], v[36:39], v[88:91]\n" \
  "s_waitcnt lgkmcnt(0)\n" \
  "s_barrier\n" \
  "ds_read_b64_tr_b16 v[48:49], v122 offset:0\n" \
  "ds_read_b64_tr_b16 v[50:51], v122 offset:512\n" \
  "v_mfma_f32_16x16x32_bf16 v[84:87], v[56:59], v[40:43], v[84:87]\n" \
  "ds_read_b64_tr_b16 v[52:53], v122 offset:1024\n" \
  "ds_read_b64_tr_b16 v[54:55], v122 offset:1536\n" \
  "v_mfma_f32_16x16x32_bf16 v[88:91], v[72:75], v[40:43], v[88:91]\n" \
  "ds_read_b64_tr_b16 v[64:65], v122 offset:2048\n" \
  "ds_read_b64_tr_b16 v[66:67], v122 offset:2560\n" \
  "v_mfma_f32_16x16x32_bf16 v[84:87], v[60:63], v[44:47], v[84:87]\n" \
  "ds_read_b64_tr_b16 v[68:69], v122 offset:3072\n" \
  "ds_read_b64_tr_b16 v[70:71], v122 offset:3584\n" \
  "v_mfma_f32_16x16x32_bf16 v[88:91], v[76:79], v[44:47], v[88:91]\n" \
  "s_waitcnt lgkmcnt(6)\n" \
  "v_mfma_f32_16x16x32_bf16 v[104:107], v[48:51], v[32:35], 0\n" \
  "v_mov_b64 v[96:97], s[12:13]\n" \
  "v_mov_b64 v[98:99], s[12:13]\n" \
  "s_waitcnt lgkmcnt(4)\n" \
  "v_mfma_f32_16x16x32_bf16 v[104:107], v[52:55], v[36:39], v[104:107]\n" \
  "s_waitcnt lgkmcnt(2)\n" \
  "v_mfma_f32_16x16x32_bf16 v[104:107], v[64:67], v[40:43], v[104:107]\n" \
  "v_cndmask_b32 v56, v84, v88, s[60:61]\n" \
  "v_cndmask_b32 v57, v85, v89, s[60:61]\n" \
  "s_waitcnt lgkmcnt(0)\n" \
  "v_mfma_f32_16x16x32_bf16 v[104:107], v[68:71], v[44:47], v[104:107]\n" \
  "v_cndmask_b32 v58, v86, v90, s[60:61]\n" \
  "v_cndmask_b32 v59, v87, v91, s[60:61]\n" \
  "v_max3_f32 v61, v56, v57, v58\n" \
  "v_max_f32 v61, v61, v59\n" \
  "v_smfmac_f32_16x16x64_bf16 v[100:103], v[8:11], a[144:151], v112\n" \
  "s_nop 0\n" \
  "v_max_f32_dpp v62, v61, v61 row_ror:8 row_mask:0xf bank_mask:0xf\n" \
  "v_max_f32_dpp v61, v61, v61 row_ror:8 row_mask:0xf bank_mask:0xf\n" \
  "v_smfmac_f32_16x16x64_bf16 v[100:103], v[12:15], a[152:159], v112\n" \
  "s_nop 0\n" \
  "v_permlane16_swap_b32 v61, v62\n" \
  "v_max3_f32 v63, v104, v105, v106\n" \
  "v_max3_f32 v61, v61, v62, v107\n" \
  "v_smfmac_f32_16x16x64_bf16 v[100:103], v[16:19], a[160:167], v112\n" \
  "v_max_f32 v62, v61, v63\n" \
  "v_max_f32 v61, v61, v63\n" \
  "v_cndmask_b32 v60, v104, v105, s[60:61]\n" \
  "v_smfmac_f32_16x16x64_bf16 v[100:103], v[20:23], a[168:175], v112\n" \
  "v_permlane32_swap_b32 v61, v62\n" \
  "v_max_f32 v61, v61, v62\n" \
  "v_mul_f32 v63, s99, v61\n" \
  "v_smfmac_f32_16x16x64_bf16 v[100:103], v[24:27], a[176:183], v112\n" \
  "v_fma_f32 v56, v56, s98, v63\n" \
  "v_fma_f32 v57, v57, s98, v63\n" \
  "v_smfmac_f32_16x16x64_bf16 v[100:103], v[28:31], a[184:191], v112\n" \
  "v_fma_f32 v58, v58, s98, v63\n" \
  "v_fma_f32 v59, v59, s98, v63\n" \
  "v_fma_f32 v60, v60, s98, v63\n" \
  "v_exp_f32 v56, v56\n" \
  "v_exp_f32 v57, v57\n" \
  "v_exp_f32 v58, v58\n" \
  "v_exp_f32 v59, v59\n" \
  "v_exp_f32 v60, v60\n" \
  "v_cvt_pk_bf16_f32 v92, v56, v57\n" \
  "v_cvt_pk_bf16_f32 v93, v58, v59\n" \
  "v_cvt_pk_bf16_f32 v94, v60, 0\n" \
  "v_add_f32 v100, v100, v102\n" \
  "v_add_f32 v101, v101, v103\n" \
  "v_mfma_f32_16x16x32_bf16 v[88:91], v[96:99], v[92:95], 0\n" \
  "v_cvt_pk_bf16_f32 v82, v100, v101\n" \
  "v_cvt_pk_bf16_f32 v110, v101, v100\n" \
  "v_mov_b64 v[0:1], 0\n" \
  "v_mfma_f32_16x16x32_bf16 v[104:107], v[80:83], v[92:95], 0\n" \
  "v_mfma_f32_16x16x32_bf16 v[84:87], v[108:111], v[92:95], 0\n" \
  "buffer_load_dwordx2 v[44:45], v125, s[36:39], s32 offen sc1\n" \
  "ds_write_b32 v114, v82\n" \
  "v_mov_b64 v[2:3], 0\n" \
  "v_add_f32_dpp v88, v88, v88 row_ror:8 row_mask:0xf bank_mask:0xf\n" \
  "v_rcp_f32 v88, v88\n" \
  "v_mov_b64 v[4:5], 0\n" \
  "v_mov_b64 v[6:7], 0\n" \
  "v_mov_b64 v[8:9], 0\n" \
  "v_add_f32_dpp v104, v84, v104 row_ror:8 row_mask:0xf bank_mask:0xf\n" \
  "v_add_f32_dpp v105, v85, v105 row_ror:8 row_mask:0xf bank_mask:0xf\n" \
  "v_add_f32_dpp v106, v86, v106 row_ror:8 row_mask:0xf bank_mask:0xf\n" \
  "v_add_f32_dpp v107, v87, v107 row_ror:8 row_mask:0xf bank_mask:0xf\n" \
  "v_mul_f32 v104, v104, v88\n" \
  "v_mul_f32 v105, v105, v88\n" \
  "v_mul_f32 v106, v106, v88\n" \
  "v_mul_f32 v107, v107, v88\n" \
  "v_cvt_pk_bf16_f32 v104, v104, v105\n" \
  "v_cvt_pk_bf16_f32 v105, v106, v107\n" \
  "ds_write_b64 v126, v[104:105]\n" \
  "v_mov_b64 v[10:11], 0\n" \
  "v_mov_b64 v[12:13], 0\n" \
  "v_mov_b64 v[14:15], 0\n" \
  "s_waitcnt lgkmcnt(0)\n" \
  "s_barrier\n" \
  "ds_read_b128 v[40:43], v127\n" \
  "ds_read_b128 v[16:19], v127 offset:64\n" \
  "s_cbranch_scc1 L_ns2_%=\n" \
  "s_mov_b32 s69, 0\n" \
  "s_add_u32 s15, s15, 4\n" \
  "s_waitcnt lgkmcnt(1)\n" \
  "v_smfmac_f32_16x16x64_bf16 v[0:3], v[40:43], a[192:199], v112\n" \
  "v_smfmac_f32_16x16x64_bf16 v[4:7], v[40:43], a[208:215], v112\n" \
  "s_waitcnt lgkmcnt(0)\n" \
  "v_smfmac_f32_16x16x64_bf16 v[0:3], v[16:19], a[200:207], v112\n" \
  "v_smfmac_f32_16x16x64_bf16 v[4:7], v[16:19], a[216:223], v112\n" \
  "v_smfmac_f32_16x16x64_bf16 v[8:11], v[40:43], a[224:231], v112\n" \
  "s_nop 1\n" \
  "v_smfmac_f32_16x16x64_bf16 v[12:15], v[40:43], a[240:247], v112\n" \
  "s_nop 1\n" \
  "v_smfmac_f32_16x16x64_bf16 v[8:11], v[16:19], a[232:239], v112\n" \
  "v_cvt_pk_bf16_f32 v32, v0, v1\n" \
  "v_smfmac_f32_16x16x64_bf16 v[12:15], v[16:19], a[248:255], v112\n" \
  "v_cvt_pk_bf16_f32 v33, v2, v3\n" \
  "v_cvt_pk_bf16_f32 v34, v4, v5\n" \
  "v_cvt_pk_bf16_f32 v35, v6, v7\n" \
  "buffer_store_dwordx4 v[32:35], v117, s[36:39], s48 offen sc1\n" \
  "s_nop 1\n" \
  "v_cvt_pk_bf16_f32 v36, v8, v9\n" \
  "v_cvt_pk_bf16_f32 v37, v10, v11\n" \
  "v_cvt_pk_bf16_f32 v38, v12, v13\n" \
  "v_cvt_pk_bf16_f32 v39, v14, v15\n" \
  "buffer_store_dwordx4 v[36:39], v117, s[36:39], s44 offen sc1\n" \
  "s_waitcnt vmcnt(2)\n" \
  "L_stchk_%=:\n" \
  "s_memtime s[64:65]\n" \
  "v_cmp_eq_u32 vcc, s26, v44\n" \
  "v_max_u32_dpp v44, v44, v44 row_ror:1 row_mask:0xf bank_mask:0xf\n" \
  "v_max_u32_dpp v45, v45, v45 row_ror:1 row_mask:0xf bank_mask:0xf\n" \
  "s_nop 0\n" \
  "v_max_u32_dpp v44, v44, v44 row_ror:2 row_mask:0xf bank_mask:0xf\n" \
  "v_max_u32_dpp v45, v45, v45 row_ror:2 row_mask:0xf bank_mask:0xf\n" \
  "s_nop 0\n" \
  "v_max_u32_dpp v44, v44, v44 row_ror:4 row_mask:0xf bank_mask:0xf\n" \
  "v_max_u32_dpp v45, v45, v45 row_ror:4 row_mask:0xf bank_mask:0xf\n" \
  "s_nop 0\n" \
  "v_max_u32_dpp v44, v44, v44 row_ror:8 row_mask:0xf bank_mask:0xf\n" \
  "v_max_u32_dpp v45, v45, v45 row_ror:8 row_mask:0xf bank_mask:0xf\n" \
  "s_cbranch_vccnz L_strep_%=\n" \
  "L_stok_%=:\n" \
  "v_readlane_b32 s73, v44, 0\n" \
  "v_readlane_b32 s66, v44, 16\n" \
  "v_readlane_b32 s70, v44, 32\n" \
  "v_readlane_b32 s71, v44, 48\n" \
  "s_max_u32 s73, s73, s66\n" \
  "s_max_u32 s70, s70, s71\n" \
  "s_max_u32 s73, s73, s70\n" \
  "s_sub_u32 s73, s73, s1\n" \
  "s_mul_i32 s73, s73, s3\n" \
  "s_ashr_i32 s73, s73, 12\n" \
  "s_add_u32 s73, s73, s0\n" \
  "s_cmp_lg_u32 s54, 0\n" \
  "s_cbranch_scc1 L_nc_%=\n" \
  "s_waitcnt lgkmcnt(0)\n" \
  "s_sub_u32 s21, s64, s17\n" \
  "s_add_u32 s64, s64, 0x64\n" \
  "L_spbe_%=:\n" \
  "s_sub_u32 s66, s73, s64\n" \
  "s_sub_u32 s66, s66, 0xa0\n" \
  "s_cmp_lt_u32 s66, 0x2000\n" \
  "s_cbranch_scc1 L_spbw_%=\n" \
  "s_sub_u32 s66, 12, s66\n" \
  "s_min_u32 s66, s66, 0xac\n" \
  "s_and_b32 s66, s66, -4\n" \
  "s_getpc_b64 s[70:71]\n" \
  "s_add_u32 s70, s70, s66\n" \
  "s_addc_u32 s71, s71, 0\n" \
  "s_setpc_b64 s[70:71]\n" \
  "s_nop 0\n" \
  "s_nop 0\n" \
  "s_nop 0\n" \
  "s_nop 0\n" \
  "s_nop 0\n" \
  "s_nop 0\n" \
  "s_nop 0\n" \
  "s_nop 0\n" \
  "s_nop 0\n" \
  "s_nop 0\n" \
  "s_nop 0\n" \
  "s_nop 0\n" \
  "s_nop 0\n" \
  "s_nop 0\n" \
  "s_nop 0\n" \
  "s_nop 0\n" \
  "s_nop 0\n" \
  "s_nop 0\n" \
  "s_nop 0\n" \
  "s_nop 0\n" \
  "s_nop 0\n" \
  "s_nop 0\n" \
  "s_nop 0\n" \
  "s_nop 0\n" \
  "s_nop 0\n" \
  "s_nop 0\n" \
  "s_nop 0\n" \
  "s_nop 0\n" \
  "s_nop 0\n" \
  "s_nop 0\n" \
  "s_nop 0\n" \
  "s_nop 0\n" \
  "s_nop 0\n" \
  "s_nop 0\n" \
  "s_nop 0\n" \
  "s_nop 0\n" \
  "s_nop 0\n" \
  "s_nop 0\n" \
  "s_nop 0\n" \
  "s_nop 0\n" \
  "buffer_load_dwordx4 v[0:3], v113, s[36:39], s45 offen offset:0 sc1\n" \
  "buffer_load_dwordx4 v[4:7], v113, s[36:39], s45 offen offset:1024 sc1\n" \
  "buffer_load_dwordx4 v[8:11], v113, s[36:39], s45 offen offset:2048 sc1\n" \
  "buffer_load_dwordx4 v[12:15], v113, s[36:39], s45 offen offset:3072 sc1\n" \
  "buffer_load_dwordx4 v[16:19], v113, s[36:39], s46 offen offset:0 sc1\n" \
  "buffer_load_dwordx4 v[20:23], v113, s[36:39], s46 offen offset:1024 sc1\n" \
  "buffer_load_dwordx4 v[24:27], v113, s[36:39], s46 offen offset:2048 sc1\n" \
  "buffer_load_dwordx4 v[28:31], v113, s[36:39], s46 offen offset:3072 sc1\n" \
  "s_cmp_lg_u32 s69, 0\n" \
  "s_cbranch_scc1 L_nons_%=\n" \
  "v_lshrrev_b32 v106, 2, v113\n" \
  "v_add_u32 v106, 0x24000, v106\n" \
  "ds_read_b32 v40, v106 offset:0\n" \
  "ds_read_b32 v41, v106 offset:1024\n" \
  "ds_read_b32 v42, v106 offset:2048\n" \
  "ds_read_b32 v43, v106 offset:3072\n" \
  "s_min_u32 s15, s15, s21\n" \
  "v_readlane_b32 s64, v45, 0\n" \
  "v_readlane_b32 s65, v45, 16\n" \
  "v_readlane_b32 s66, v45, 32\n" \
  "v_readlane_b32 s75, v45, 48\n" \
  "s_max_u32 s64, s64, s65\n" \
  "s_max_u32 s75, s75, s66\n" \
  "s_max_u32 s75, s75, s64\n" \
  "s_sub_u32 s65, s75, s4\n" \
  "s_ashr_i32 s64, s65, 2\n" \
  "s_min_i32 s65, s65, 24\n" \
  "s_cmp_gt_i32 s65, 0\n" \
  "s_cselect_b32 s65, s65, s64\n" \
  "s_add_u32 s4, s4, s65\n" \
  "s_cmp_lt_u32 s33, 3\n" \
  "s_cselect_b32 s4, s75, s4\n" \
  "s_add_u32 s75, s4, s57\n" \
  "s_add_u32 s75, s75, s73\n" \
  "s_add_u32 s75, s75, s62\n" \
  "s_sub_u32 s62, s62, 24\n" \
  "s_max_i32 s62, s62, 0\n" \
  "v_add_u32 v104, s41, v113\n" \
  "v_add_u32 v105, s41, v124\n" \
  "ds_read_b128 v[48:51], v104 offset:0\n" \
  "ds_read_b128 v[52:55], v104 offset:1024\n" \
  "ds_read_b128 v[56:59], v104 offset:2048\n" \
  "ds_read_b128 v[60:63], v104 offset:3072\n" \
  "ds_read_b128 v[64:67], v104 offset:4096\n" \
  "ds_read_b128 v[68:71], v104 offset:5120\n" \
  "ds_read_b128 v[72:75], v104 offset:6144\n" \
  "ds_read_b128 v[76:79], v104 offset:7168\n" \
  "ds_read_b64 v[80:81], v105\n" \
  "ds_read_b64 v[108:109], v105 offset:8\n" \
  "s_mov_b32 s74, s41\n" \
  "s_mov_b32 s41, s42\n" \
  "s_mov_b32 s42, s43\n" \
  "s_mov_b32 s43, s74\n" \
  "v_mov_b64 v[88:89], 0\n" \
  "v_mov_b64 v[90:91], 0\n" \
  "v_mov_b64 v[92:93], 0\n" \
  "v_mov_b64 v[94:95], 0\n" \
  "v_mov_b64 v[96:97], 0\n" \
  "v_mov_b64 v[98:99], 0\n" \
  "v_mov_b64 v[100:101], 0\n" \
  "v_mov_b64 v[102:103], 0\n" \
  "s_add_u32 s47, s50, s67\n" \
  "s_mov_b32 s74, s50\n" \
  "s_mov_b32 s50, s48\n" \
  "s_mov_b32 s48, s49\n" \
  "s_mov_b32 s49, s74\n" \
  "s_add_u32 s33, s33, 1\n" \
  "s_lshr_b32 s32, s33, 3\n" \
  "s_lshl_b32 s32, s32, 18\n" \
  "s_add_u32 s32, s32, s56\n" \
  "s_and_b32 s74, s33, 7\n" \
  "s_add_u32 s74, s74, 1\n" \
  "s_lshl_b32 s74, s74, 14\n" \
  "s_add_u32 s34, s32, s74\n" \
  "s_lshl_b32 s35, s54, 10\n" \
  "s_add_u32 s35, s35, s34\n" \
  "s_add_u32 s40, s35, 0x1000\n" \
  "s_and_b32 s74, s33, 3\n" \
  "s_lshl_b32 s74, s74, 11\n" \
  "s_add_u32 s32, s32, s74\n" \
  "s_and_b32 s74, s33, 4\n" \
  "s_lshl_b32 s74, s74, 12\n" \
  "s_add_u32 s32, s32, s74\n" \
  "s_add_u32 s32, s32, 0x24000\n" \
  "s_add_u32 s44, s48, 0x40000\n" \
  "s_waitcnt lgkmcnt(0)\n" \
  "L_nons_%=:\n" \
  "s_waitcnt vmcnt(7)\n" \
  "v_mfma_f32_16x16x32_bf16 v[84:87], v[0:3], v[40:43], 0\n" \
  "s_waitcnt vmcnt(6)\n" \
  "v_mfma_f32_16x16x32_bf16 v[84:87], v[4:7], v[40:43], v[84:87]\n" \
  "s_waitcnt vmcnt(5)\n" \
  "v_mfma_f32_16x16x32_bf16 v[84:87], v[8:11], v[40:43], v[84:87]\n" \
  "s_waitcnt vmcnt(4)\n" \
  "v_mfma_f32_16x16x32_bf16 v[84:87], v[12:15], v[40:43], v[84:87]\n" \
  "s_waitcnt vmcnt(3)\n" \
  "v_mfma_f32_16x16x32_bf16 v[84:87], v[16:19], v[40:43], v[84:87]\n" \
  "s_waitcnt vmcnt(2)\n" \
  "v_mfma_f32_16x16x32_bf16 v[84:87], v[20:23], v[40:43], v[84:87]\n" \
  "s_waitcnt vmcnt(1)\n" \
  "v_mfma_f32_16x16x32_bf16 v[84:87], v[24:27], v[40:43], v[84:87]\n" \
  "s_waitcnt vmcnt(0)\n" \
  "v_mfma_f32_16x16x32_bf16 v[84:87], v[28:31], v[40:43], v[84:87]\n" \
  "s_nop 7\n" \
  "v_permlane32_swap_b32 v84, v86\n" \
  "v_permlane32_swap_b32 v85, v87\n" \
  "v_add_f32 v84, v84, v86\n" \
  "v_add_f32 v85, v85, v87\n" \
  "v_cmp_u_f32 vcc, v84, v85\n" \
  "v_cvt_pk_bf16_f32 v84, v84, v85\n" \
  "s_cbranch_vccnz L_slowb_%=\n" \
  "L_contb_%=:\n" \
  "buffer_store_dword v84, v118, s[36:39], s34 offen sc1\n" \
  "s_memtime s[64:65]\n" \
  "s_waitcnt lgkmcnt(0)\n" \
  "s_sub_u32 s16, s64, s73\n" \
  "s_mov_b32 s73, s75\n" \
  "L_spae_%=:\n" \
  "s_sub_u32 s66, s73, s64\n" \
  "s_sub_u32 s66, s66, 0xa0\n" \
  "s_cmp_lt_u32 s66, 0x2000\n" \
  "s_cbranch_scc1 L_spaw_%=\n" \
  "s_sub_u32 s66, 12, s66\n" \
  "s_min_u32 s66, s66, 0xac\n" \
  "s_and_b32 s66, s66, -4\n" \
  "s_getpc_b64 s[70:71]\n" \
  "s_add_u32 s70, s70, s66\n" \
  "s_addc_u32 s71, s71, 0\n" \
  "s_setpc_b64 s[70:71]\n" \
  "s_nop 0\n" \
  "s_nop 0\n" \
  "s_nop 0\n" \
  "s_nop 0\n" \
  "s_nop 0\n" \
  "s_nop 0\n" \
  "s_nop 0\n" \
  "s_nop 0\n" \
  "s_nop 0\n" \
  "s_nop 0\n" \
  "s_nop 0\n" \
  "s_nop 0\n" \
  "s_nop 0\n" \
  "s_nop 0\n" \
  "s_nop 0\n" \
  "s_nop 0\n" \
  "s_nop 0\n" \
  "s_nop 0\n" \
  "s_nop 0\n" \
  "s_nop 0\n" \
  "s_nop 0\n" \
  "s_nop 0\n" \
  "s_nop 0\n" \
  "s_nop 0\n" \
  "s_nop 0\n" \
  "s_nop 0\n" \
  "s_nop 0\n" \
  "s_nop 0\n" \
  "s_nop 0\n" \
  "s_nop 0\n" \
  "s_nop 0\n" \
  "s_nop 0\n" \
  "s_nop 0\n" \
  "s_nop 0\n" \
  "s_nop 0\n" \
  "s_nop 0\n" \
  "s_nop 0\n" \
  "s_nop 0\n" \
  "s_nop 0\n" \
  "s_nop 0\n" \
  "s_mov_b32 m0, s68\n" \
  "s_nop 0\n" \
  "buffer_load_dwordx4 v113, s[36:39], s35 offen sc1 lds\n" \
  "s_xor_b32 m0, m0, 0x1000\n" \
  "s_nop 0\n" \
  "buffer_load_dwordx4 v113, s[36:39], s40 offen sc1 lds\n" \
  "v_mov_b32 v4, 0x2c00\n" \
  "ds_read_b128 v[0:3], v4\n" \
  "v_mov_b64 v[84:85], 0\n" \
  "v_mov_b64 v[86:87], 0\n" \
  "s_add_u32 s45, s48, s55\n" \
  "s_add_u32 s46, s45, 0x1000\n" \
  "s_waitcnt lgkmcnt(0)\n" \
  "v_readfirstlane_b32 s0, v0\n" \
  "v_readfirstlane_b32 s1, v1\n" \
  "v_readfirstlane_b32 s2, v2\n" \
  "v_readfirstlane_b32 s3, v3\n" \
  "s_sub_u32 s53, s15, s0\n" \
  "v_xor_b32 v37, 0x400, v116\n" \
  "v_xor_b32 v38, 0x800, v116\n" \
  "v_xor_b32 v39, 0xc00, v116\n" \
  "v_mov_b32 v105, s16\n" \
  "v_mov_b32 v106, s18\n" \
  "v_bfrev_b32 v107, 2\n" \
  "v_cndmask_b32 v106, v107, v106, s[10:11]\n" \
  "s_waitcnt vmcnt(0)\n" \
  "s_branch L_body_%=\n" \
  "L_nc_%=:\n" \
  "s_sub_u32 s0, s0, s58\n" \
  "s_waitcnt lgkmcnt(0)\n" \
  "v_readlane_b32 s64, v45, 0\n" \
  "v_readlane_b32 s65, v45, 16\n" \
  "v_readlane_b32 s66, v45, 32\n" \
  "v_readlane_b32 s75, v45, 48\n" \
  "s_max_u32 s64, s64, s65\n" \
  "s_max_u32 s75, s75, s66\n" \
  "s_max_u32 s75, s75, s64\n" \
  "s_sub_u32 s65, s75, s4\n" \
  "s_ashr_i32 s64, s65, 2\n" \
  "s_min_i32 s65, s65, 24\n" \
  "s_cmp_gt_i32 s65, 0\n" \
  "s_cselect_b32 s65, s65, s64\n" \
  "s_add_u32 s4, s4, s65\n" \
  "s_cmp_lt_u32 s33, 3\n" \
  "s_cselect_b32 s4, s75, s4\n" \
  "s_add_u32 s75, s4, s57\n" \
  "s_add_u32 s75, s75, s73\n" \
  "s_add_u32 s75, s75, s62\n" \
  "s_sub_u32 s62, s62, 24\n" \
  "s_max_i32 s62, s62, 0\n" \
  "s_lshl_b32 s74, s54, 8\n" \
  "v_mov_b32 v24, s74\n" \
  "ds_read_b32 v24, v24 offset:13312\n" \
  "s_waitcnt lgkmcnt(0)\n" \
  "v_readfirstlane_b32 s29, v24\n" \
  "s_cmp_lg_u32 s33, 0\n" \
  "s_cbranch_scc1 L_ns2_%=\n" \
  "s_cmp_ge_u32 s29, s6\n" \
  "s_cbranch_scc1 L_kvgo1_%=\n" \
  "v_mov_b32 v24, 0\n" \
  "s_mov_b32 s71, 0\n" \
  "L_kvp1_%=:\n" \
  "buffer_load_dword v25, v24, s[36:39], s19 offen sc1\n" \
  "s_waitcnt vmcnt(0)\n" \
  "v_readfirstlane_b32 s29, v25\n" \
  "s_add_u32 s71, s71, 1\n" \
  "s_cmp_gt_u32 s71, 0x80000\n" \
  "s_cbranch_scc1 L_kvgo1_%=\n" \
  "s_cmp_ge_u32 s29, s6\n" \
  "s_cbranch_scc1 L_kvgo1_%=\n" \
  "s_sleep 4\n" \
  "s_branch L_kvp1_%=\n" \
  "L_kvgo1_%=:\n" \
  "s_add_u32 m0, s42, s59\n" \
  "s_lshr_b32 s74, s51, 21\n" \
  "s_xor_b32 s74, s74, s30\n" \
  "s_and_b32 s74, s74, 1\n" \
  "s_lshl_b32 s74, s74, 13\n" \
  "s_add_u32 s74, s74, s51\n" \
  "buffer_load_dwordx4 v113, s[36:39], s74 offen sc1 lds\n" \
  "s_add_u32 m0, m0, 0x400\n" \
  "s_add_u32 s74, s74, 0x400\n" \
  "buffer_load_dwordx4 v113, s[36:39], s74 offen sc1 lds\n" \
  "s_add_u32 m0, m0, 0x400\n" \
  "s_add_u32 s74, s74, 0x400\n" \
  "buffer_load_dwordx4 v113, s[36:39], s74 offen sc1 lds\n" \
  "s_add_u32 m0, m0, 0x400\n" \
  "s_add_u32 s74, s74, 0x400\n" \
  "buffer_load_dwordx4 v113, s[36:39], s74 offen sc1 lds\n" \
  "v_mov_b32 v24, 0\n" \
  "s_add_u32 s7, s7, 1\n" \
  "s_and_b32 s19, s7, 1\n" \
  "s_lshl_b32 s19, s19, 7\n" \
  "s_add_u32 s19, s19, s5\n" \
  "s_lshr_b32 s6, s7, 1\n" \
  "s_add_u32 s6, s6, 1\n" \
  "s_min_u32 s6, s6, 32\n" \
  "s_lshl_b32 m0, s54, 8\n" \
  "s_add_u32 m0, m0, 0x3400\n" \
  "s_nop 0\n" \
  "buffer_load_dword v24, s[36:39], s19 offen sc1 lds\n" \
  "s_cmp_lt_u32 s7, 64\n" \
  "s_cselect_b32 s74, 0x80000, 0\n" \
  "s_add_u32 s51, s51, s74\n" \
  "s_waitcnt vmcnt(0)\n" \
  "s_lshl_b32 s74, s54, 8\n" \
  "v_mov_b32 v24, s74\n" \
  "ds_read_b32 v24, v24 offset:13312\n" \
  "s_waitcnt lgkmcnt(0)\n" \
  "v_readfirstlane_b32 s29, v24\n" \
  "L_ns2_%=:\n" \
  "s_cmp_eq_u32 s54, 3\n" \
  "s_cbranch_scc1 L_pv2_%=\n" \
  "ds_read_b64_tr_b16 v[16:17], v123\n" \
  "ds_read_b64_tr_b16 v[18:19], v123 offset:1024\n" \
  "s_branch L_pj2_%=\n" \
  "L_pv2_%=:\n" \
  "ds_read_b128 v[16:19], v113 offset:12288\n" \
  "L_pj2_%=:\n" \
  "s_lshr_b32 s14, s52, 21\n" \
  "s_xor_b32 s14, s14, s30\n" \
  "s_and_b32 s14, s14, 1\n" \
  "s_lshl_b32 s14, s14, 13\n" \
  "s_add_u32 s14, s14, s52\n" \
  "s_add_u32 s52, s52, 0x80000\n" \
  "s_cmp_eq_u32 s33, 63\n" \
  "s_cbranch_scc1 L_pst_%=\n" \
  "v_mov_b32 v20, s26\n" \
  "v_mov_b32 v21, s26\n" \
  "v_mov_b32 v22, s26\n" \
  "v_mov_b32 v23, s26\n" \
  "v_add_u32 v104, s41, v113\n" \
  "v_add_u32 v105, s41, v124\n" \
  "ds_read_b128 v[48:51], v104 offset:0\n" \
  "ds_read_b128 v[52:55], v104 offset:1024\n" \
  "ds_read_b128 v[56:59], v104 offset:2048\n" \
  "ds_read_b128 v[60:63], v104 offset:3072\n" \
  "ds_read_b128 v[64:67], v104 offset:4096\n" \
  "ds_read_b128 v[68:71], v104 offset:5120\n" \
  "ds_read_b128 v[72:75], v104 offset:6144\n" \
  "ds_read_b128 v[76:79], v104 offset:7168\n" \
  "ds_read_b64 v[80:81], v105\n" \
  "ds_read_b64 v[108:109], v105 offset:8\n" \
  "s_mov_b32 s74, s41\n" \
  "s_mov_b32 s41, s42\n" \
  "s_mov_b32 s42, s43\n" \
  "s_mov_b32 s43, s74\n" \
  "v_mov_b64 v[88:89], 0\n" \
  "v_mov_b64 v[90:91], 0\n" \
  "v_mov_b64 v[92:93], 0\n" \
  "v_mov_b64 v[94:95], 0\n" \
  "v_mov_b64 v[96:97], 0\n" \
  "v_mov_b64 v[98:99], 0\n" \
  "v_mov_b64 v[100:101], 0\n" \
  "v_mov_b64 v[102:103], 0\n" \
  "s_add_u32 s47, s50, s67\n" \
  "s_mov_b32 s74, s50\n" \
  "s_mov_b32 s50, s48\n" \
  "s_mov_b32 s48, s49\n" \
  "s_mov_b32 s49, s74\n" \
  "s_add_u32 s33, s33, 1\n" \
  "s_lshr_b32 s32, s33, 3\n" \
  "s_lshl_b32 s32, s32, 18\n" \
  "s_add_u32 s32, s32, s56\n" \
  "s_and_b32 s74, s33, 7\n" \
  "s_add_u32 s74, s74, 1\n" \
  "s_lshl_b32 s74, s74, 14\n" \
  "s_add_u32 s34, s32, s74\n" \
  "s_lshl_b32 s35, s54, 10\n" \
  "s_add_u32 s35, s35, s34\n" \
  "s_add_u32 s40, s35, 0x1000\n" \
  "s_and_b32 s74, s33, 3\n" \
  "s_lshl_b32 s74, s74, 11\n" \
  "s_add_u32 s32, s32, s74\n" \
  "s_and_b32 s74, s33, 4\n" \
  "s_lshl_b32 s74, s74, 12\n" \
  "s_add_u32 s32, s32, s74\n" \
  "s_add_u32 s32, s32, 0x24000\n" \
  "s_add_u32 s44, s48, 0x40000\n" \
  "s_cmp_lg_u32 s54, 2\n" \
  "s_cbranch_scc1 L_nocal_%=\n" \
  "s_memtime s[64:65]\n" \
  "s_nop 0\n" \
  "s_memrealtime s[20:21]\n" \
  "s_nop 0\n" \
  "s_memrealtime s[22:23]\n" \
  "s_nop 0\n" \
  "s_memrealtime s[24:25]\n" \
  "s_nop 0\n" \
  "s_memrealtime s[76:77]\n" \
  "s_nop 0\n" \
  "s_memrealtime s[78:79]\n" \
  "s_nop 0\n" \
  "s_memrealtime s[80:81]\n" \
  "s_nop 0\n" \
  "s_memrealtime s[82:83]\n" \
  "s_nop 0\n" \
  "s_memrealtime s[84:85]\n" \
  "s_nop 0\n" \
  "s_memrealtime s[86:87]\n" \
  "s_nop 0\n" \
  "s_memrealtime s[88:89]\n" \
  "s_nop 0\n" \
  "s_memrealtime s[90:91]\n" \
  "s_nop 0\n" \
  "s_memrealtime s[92:93]\n" \
  "s_nop 0\n" \
  "s_memrealtime s[94:95]\n" \
  "s_mov_b32 s66, 0\n" \
  "s_waitcnt lgkmcnt(0)\n" \
  "s_cmp_eq_u32 s22, s20\n" \
  "s_addc_u32 s66, s66, 0\n" \
  "s_cmp_eq_u32 s24, s20\n" \
  "s_addc_u32 s66, s66, 0\n" \
  "s_cmp_eq_u32 s76, s20\n" \
  "s_addc_u32 s66, s66, 0\n" \
  "s_cmp_eq_u32 s78, s20\n" \
  "s_addc_u32 s66, s66, 0\n" \
  "s_cmp_eq_u32 s80, s20\n" \
  "s_addc_u32 s66, s66, 0\n" \
  "s_cmp_eq_u32 s82, s20\n" \
  "s_addc_u32 s66, s66, 0\n" \
  "s_cmp_eq_u32 s84, s20\n" \
  "s_addc_u32 s66, s66, 0\n" \
  "s_cmp_eq_u32 s86, s20\n" \
  "s_addc_u32 s66, s66, 0\n" \
  "s_cmp_eq_u32 s88, s20\n" \
  "s_addc_u32 s66, s66, 0\n" \
  "s_cmp_eq_u32 s90, s20\n" \
  "s_addc_u32 s66, s66, 0\n" \
  "s_cmp_eq_u32 s92, s20\n" \
  "s_addc_u32 s66, s66, 0\n" \
  "s_cmp_eq_u32 s94, s20\n" \
  "s_addc_u32 s66, s66, 0\n" \
  "s_lshl_b32 s66, s66, 3\n" \
  "s_add_u32 s64, s64, s66\n" \
  "s_sub_u32 s65, s20, s31\n" \
  "s_add_u32 s65, s65, 4\n" \
  "s_lshl_b32 s65, s65, 5\n" \
  "s_sub_u32 s66, s64, s0\n" \
  "s_sub_u32 s70, s65, s1\n" \
  "v_cvt_f32_u32 v0, s66\n" \
  "v_cvt_f32_u32 v1, s70\n" \
  "v_rcp_f32 v2, v0\n" \
  "v_rcp_f32 v3, v1\n" \
  "v_mul_f32 v0, 0x45800000, v0\n" \
  "v_mul_f32 v1, 0x45800000, v1\n" \
  "v_fma_f32 v2, v2, v1, 0.5\n" \
  "v_fma_f32 v3, v3, v0, 0.5\n" \
  "v_cvt_u32_f32 v2, v2\n" \
  "v_cvt_u32_f32 v3, v3\n" \
  "v_readfirstlane_b32 s66, v2\n" \
  "v_readfirstlane_b32 s70, v3\n" \
  "s_max_u32 s66, s66, 0x1324\n" \
  "s_min_u32 s66, s66, 0x2af8\n" \
  "s_max_u32 s70, s70, 0x5f5\n" \
  "s_min_u32 s70, s70, 0xd60\n" \
  "s_add_u32 s2, s2, s66\n" \
  "s_add_u32 s2, s2, 1\n" \
  "s_lshr_b32 s2, s2, 1\n" \
  "s_add_u32 s3, s3, s70\n" \
  "s_add_u32 s3, s3, 1\n" \
  "s_lshr_b32 s3, s3, 1\n" \
  "s_mov_b32 s0, s64\n" \
  "s_mov_b32 s1, s65\n" \
  "v_mov_b32 v0, s0\n" \
  "v_mov_b32 v1, s1\n" \
  "v_mov_b32 v2, s2\n" \
  "v_mov_b32 v3, s3\n" \
  "v_mov_b32 v4, 0x2c00\n" \
  "ds_write_b128 v4, v[0:3]\n" \
  "L_nocal_%=:\n" \
  "s_cmp_ge_u32 s29, s6\n" \
  "s_cbranch_scc1 L_kvgo3_%=\n" \
  "v_mov_b32 v24, 0\n" \
  "s_mov_b32 s71, 0\n" \
  "L_kvp3_%=:\n" \
  "buffer_load_dword v25, v24, s[36:39], s19 offen sc1\n" \
  "s_waitcnt vmcnt(0)\n" \
  "v_readfirstlane_b32 s29, v25\n" \
  "s_add_u32 s71, s71, 1\n" \
  "s_cmp_gt_u32 s71, 0x80000\n" \
  "s_cbranch_scc1 L_kvgo3_%=\n" \
  "s_cmp_ge_u32 s29, s6\n" \
  "s_cbranch_scc1 L_kvgo3_%=\n" \
  "s_sleep 4\n" \
  "s_branch L_kvp3_%=\n" \
  "L_kvgo3_%=:\n" \
  "s_sub_u32 s73, s75, 0x2bc\n" \
  "L_sprw_%=:\n" \
  "s_memtime s[64:65]\n" \
  "s_waitcnt lgkmcnt(0)\n" \
  "L_spre_%=:\n" \
  "s_sub_u32 s66, s73, s64\n" \
  "s_sub_u32 s66, s66, 0xa0\n" \
  "s_cmp_lt_u32 s66, 0x2000\n" \
  "s_cbranch_scc1 L_sprw_%=\n" \
  "s_sub_u32 s66, 12, s66\n" \
  "s_min_u32 s66, s66, 0xac\n" \
  "s_and_b32 s66, s66, -4\n" \
  "s_getpc_b64 s[70:71]\n" \
  "s_add_u32 s70, s70, s66\n" \
  "s_addc_u32 s71, s71, 0\n" \
  "s_setpc_b64 s[70:71]\n" \
  "s_nop 0\n" \
  "s_nop 0\n" \
  "s_nop 0\n" \
  "s_nop 0\n" \
  "s_nop 0\n" \
  "s_nop 0\n" \
  "s_nop 0\n" \
  "s_nop 0\n" \
  "s_nop 0\n" \
  "s_nop 0\n" \
  "s_nop 0\n" \
  "s_nop 0\n" \
  "s_nop 0\n" \
  "s_nop 0\n" \
  "s_nop 0\n" \
  "s_nop 0\n" \
  "s_nop 0\n" \
  "s_nop 0\n" \
  "s_nop 0\n" \
  "s_nop 0\n" \
  "s_nop 0\n" \
  "s_nop 0\n" \
  "s_nop 0\n" \
  "s_nop 0\n" \
  "s_nop 0\n" \
  "s_nop 0\n" \
  "s_nop 0\n" \
  "s_nop 0\n" \
  "s_nop 0\n" \
  "s_nop 0\n" \
  "s_nop 0\n" \
  "s_nop 0\n" \
  "s_nop 0\n" \
  "s_nop 0\n" \
  "s_nop 0\n" \
  "s_nop 0\n" \
  "s_nop 0\n" \
  "s_nop 0\n" \
  "s_nop 0\n" \
  "s_nop 0\n" \
  "s_cmp_eq_u32 s54, 9\n" \
  "L_pst_%=:\n" \
  "s_waitcnt lgkmcnt(0)\n" \
  "buffer_store_dwordx4 v[16:19], v113, s[36:39], s14 offen sc1\n" \
  "s_cbranch_scc0 L_pon_%=\n" \
  "s_waitcnt vmcnt(0)\n" \
  "s_endpgm\n" \
  "L_pon_%=:\n" \
  "buffer_store_dwordx4 v[20:23], v113, s[36:39], s47 offen offset:0 sc1\n" \
  "buffer_store_dwordx4 v[20:23], v113, s[36:39], s47 offen offset:1024 sc1\n" \
  "buffer_store_dwordx4 v[20:23], v113, s[36:39], s47 offen offset:2048 sc1\n" \
  "s_mov_b32 s73, s75\n" \
  "L_spnw_%=:\n" \
  "s_memtime s[64:65]\n" \
  "s_waitcnt lgkmcnt(0)\n" \
  "L_spne_%=:\n" \
  "s_sub_u32 s66, s73, s64\n" \
  "s_sub_u32 s66, s66, 0xa0\n" \
  "s_cmp_lt_u32 s66, 0x2000\n" \
  "s_cbranch_scc1 L_spnw_%=\n" \
  "s_sub_u32 s66, 12, s66\n" \
  "s_min_u32 s66, s66, 0xac\n" \
  "s_and_b32 s66, s66, -4\n" \
  "s_getpc_b64 s[70:71]\n" \
  "s_add_u32 s70, s70, s66\n" \
  "s_addc_u32 s71, s71, 0\n" \
  "s_setpc_b64 s[70:71]\n" \
  "s_nop 0\n" \
  "s_nop 0\n" \
  "s_nop 0\n" \
  "s_nop 0\n" \
  "s_nop 0\n" \
  "s_nop 0\n" \
  "s_nop 0\n" \
  "s_nop 0\n" \
  "s_nop 0\n" \
  "s_nop 0\n" \
  "s_nop 0\n" \
  "s_nop 0\n" \
  "s_nop 0\n" \
  "s_nop 0\n" \
  "s_nop 0\n" \
  "s_nop 0\n" \
  "s_nop 0\n" \
  "s_nop 0\n" \
  "s_nop 0\n" \
  "s_nop 0\n" \
  "s_nop 0\n" \
  "s_nop 0\n" \
  "s_nop 0\n" \
  "s_nop 0\n" \
  "s_nop 0\n" \
  "s_nop 0\n" \
  "s_nop 0\n" \
  "s_nop 0\n" \
  "s_nop 0\n" \
  "s_nop 0\n" \
  "s_nop 0\n" \
  "s_nop 0\n" \
  "s_nop 0\n" \
  "s_nop 0\n" \
  "s_nop 0\n" \
  "s_nop 0\n" \
  "s_nop 0\n" \
  "s_nop 0\n" \
  "s_nop 0\n" \
  "s_nop 0\n" \
  "s_mov_b32 m0, s68\n" \
  "s_nop 0\n" \
  "buffer_load_dwordx4 v113, s[36:39], s35 offen sc1 lds\n" \
  "s_xor_b32 m0, m0, 0x1000\n" \
  "s_nop 0\n" \
  "buffer_load_dwordx4 v113, s[36:39], s40 offen sc1 lds\n" \
  "s_add_u32 m0, s42, s59\n" \
  "s_lshr_b32 s74, s51, 21\n" \
  "s_xor_b32 s74, s74, s30\n" \
  "s_and_b32 s74, s74, 1\n" \
  "s_lshl_b32 s74, s74, 13\n" \
  "s_add_u32 s74, s74, s51\n" \
  "buffer_load_dwordx4 v113, s[36:39], s74 offen sc1 lds\n" \
  "s_add_u32 m0, m0, 0x400\n" \
  "s_add_u32 s74, s74, 0x400\n" \
  "buffer_load_dwordx4 v113, s[36:39], s74 offen sc1 lds\n" \
  "s_add_u32 m0, m0, 0x400\n" \
  "s_add_u32 s74, s74, 0x400\n" \
  "buffer_load_dwordx4 v113, s[36:39], s74 offen sc1 lds\n" \
  "s_add_u32 m0, m0, 0x400\n" \
  "s_add_u32 s74, s74, 0x400\n" \
  "buffer_load_dwordx4 v113, s[36:39], s74 offen sc1 lds\n" \
  "v_mov_b32 v24, 0\n" \
  "s_add_u32 s7, s7, 1\n" \
  "s_and_b32 s19, s7, 1\n" \
  "s_lshl_b32 s19, s19, 7\n" \
  "s_add_u32 s19, s19, s5\n" \
  "s_lshr_b32 s6, s7, 1\n" \
  "s_add_u32 s6, s6, 1\n" \
  "s_min_u32 s6, s6, 32\n" \
  "s_lshl_b32 m0, s54, 8\n" \
  "s_add_u32 m0, m0, 0x3400\n" \
  "s_nop 0\n" \
  "buffer_load_dword v24, s[36:39], s19 offen sc1 lds\n" \
  "s_cmp_lt_u32 s7, 64\n" \
  "s_cselect_b32 s74, 0x80000, 0\n" \
  "s_add_u32 s51, s51, s74\n" \
  "s_sub_u32 s14, s33, 1\n" \
  "v_mov_b32 v26, s14\n" \
  "v_mov_b32 v27, 0\n" \
  "s_mov_b64 exec, 1\n" \
  "buffer_store_dword v26, v27, s[36:39], s28 offen sc1\n" \
  "s_mov_b64 exec, -1\n" \
  "v_mov_b32 v4, 0x2c00\n" \
  "ds_read_b128 v[0:3], v4\n" \
  "v_mov_b64 v[84:85], 0\n" \
  "v_mov_b64 v[86:87], 0\n" \
  "s_add_u32 s45, s48, s55\n" \
  "s_add_u32 s46, s45, 0x1000\n" \
  "s_waitcnt lgkmcnt(0)\n" \
  "v_readfirstlane_b32 s0, v0\n" \
  "v_readfirstlane_b32 s1, v1\n" \
  "v_readfirstlane_b32 s2, v2\n" \
  "v_readfirstlane_b32 s3, v3\n" \
  "s_sub_u32 s53, s15, s0\n" \
  "v_xor_b32 v37, 0x400, v116\n" \
  "v_xor_b32 v38, 0x800, v116\n" \
  "v_xor_b32 v39, 0xc00, v116\n" \
  "v_mov_b32 v105, s16\n" \
  "v_mov_b32 v106, s18\n" \
  "v_bfrev_b32 v107, 2\n" \
  "v_cndmask_b32 v106, v107, v106, s[10:11]\n" \
  "s_waitcnt vmcnt(6)\n" \
  "s_branch L_body_%=\n" \
  "L_spbw_%=:\n" \
  "s_memtime s[64:65]\n" \
  "s_waitcnt lgkmcnt(0)\n" \
  "s_add_u32 s64, s64, 24\n" \
  "s_branch L_spbe_%=\n" \
  "L_spaw_%=:\n" \
  "s_memtime s[64:65]\n" \
  "s_waitcnt lgkmcnt(0)\n" \
  "s_add_u32 s64, s64, 24\n" \
  "s_branch L_spae_%=\n" \
  "L_slowb_%=:\n" \
  "s_nop 7\n" \
  "s_add_u32 s69, s69, 1\n" \
  "s_cmp_lt_u32 s69, 3\n" \
  "s_cbranch_scc1 L_slowbr_%=\n" \
  "s_mov_b64 s[70:71], 0\n" \
  "v_cmp_eq_u32 vcc, s26, v0\n" \
  "s_nop 0\n" \
  "s_or_b64 s[70:71], s[70:71], vcc\n" \
  "v_cmp_eq_u32 vcc, s26, v1\n" \
  "s_nop 0\n" \
  "s_or_b64 s[70:71], s[70:71], vcc\n" \
  "v_cmp_eq_u32 vcc, s26, v2\n" \
  "s_nop 0\n" \
  "s_or_b64 s[70:71], s[70:71], vcc\n" \
  "v_cmp_eq_u32 vcc, s26, v3\n" \
  "s_nop 0\n" \
  "s_or_b64 s[70:71], s[70:71], vcc\n" \
  "v_cmp_eq_u32 vcc, s26, v4\n" \
  "s_nop 0\n" \
  "s_or_b64 s[70:71], s[70:71], vcc\n" \
  "v_cmp_eq_u32 vcc, s26, v5\n" \
  "s_nop 0\n" \
  "s_or_b64 s[70:71], s[70:71], vcc\n" \
  "v_cmp_eq_u32 vcc, s26, v6\n" \
  "s_nop 0\n" \
  "s_or_b64 s[70:71], s[70:71], vcc\n" \
  "v_cmp_eq_u32 vcc, s26, v7\n" \
  "s_nop 0\n" \
  "s_or_b64 s[70:71], s[70:71], vcc\n" \
  "v_cmp_eq_u32 vcc, s26, v8\n" \
  "s_nop 0\n" \
  "s_or_b64 s[70:71], s[70:71], vcc\n" \
  "v_cmp_eq_u32 vcc, s26, v9\n" \
  "s_nop 0\n" \
  "s_or_b64 s[70:71], s[70:71], vcc\n" \
  "v_cmp_eq_u32 vcc, s26, v10\n" \
  "s_nop 0\n" \
  "s_or_b64 s[70:71], s[70:71], vcc\n" \
  "v_cmp_eq_u32 vcc, s26, v11\n" \
  "s_nop 0\n" \
  "s_or_b64 s[70:71], s[70:71], vcc\n" \
  "v_cmp_eq_u32 vcc, s26, v12\n" \
  "s_nop 0\n" \
  "s_or_b64 s[70:71], s[70:71], vcc\n" \
  "v_cmp_eq_u32 vcc, s26, v13\n" \
  "s_nop 0\n" \
  "s_or_b64 s[70:71], s[70:71], vcc\n" \
  "v_cmp_eq_u32 vcc, s26, v14\n" \
  "s_nop 0\n" \
  "s_or_b64 s[70:71], s[70:71], vcc\n" \
  "v_cmp_eq_u32 vcc, s26, v15\n" \
  "s_nop 0\n" \
  "s_or_b64 s[70:71], s[70:71], vcc\n" \
  "v_cmp_eq_u32 vcc, s26, v16\n" \
  "s_nop 0\n" \
  "s_or_b64 s[70:71], s[70:71], vcc\n" \
  "v_cmp_eq_u32 vcc, s26, v17\n" \
  "s_nop 0\n" \
  "s_or_b64 s[70:71], s[70:71], vcc\n" \
  "v_cmp_eq_u32 vcc, s26, v18\n" \
  "s_nop 0\n" \
  "s_or_b64 s[70:71], s[70:71], vcc\n" \
  "v_cmp_eq_u32 vcc, s26, v19\n" \
  "s_nop 0\n" \
  "s_or_b64 s[70:71], s[70:71], vcc\n" \
  "v_cmp_eq_u32 vcc, s26, v20\n" \
  "s_nop 0\n" \
  "s_or_b64 s[70:71], s[70:71], vcc\n" \
  "v_cmp_eq_u32 vcc, s26, v21\n" \
  "s_nop 0\n" \
  "s_or_b64 s[70:71], s[70:71], vcc\n" \
  "v_cmp_eq_u32 vcc, s26, v22\n" \
  "s_nop 0\n" \
  "s_or_b64 s[70:71], s[70:71], vcc\n" \
  "v_cmp_eq_u32 vcc, s26, v23\n" \
  "s_nop 0\n" \
  "s_or_b64 s[70:71], s[70:71], vcc\n" \
  "v_cmp_eq_u32 vcc, s26, v24\n" \
  "s_nop 0\n" \
  "s_or_b64 s[70:71], s[70:71], vcc\n" \
  "v_cmp_eq_u32 vcc, s26, v25\n" \
  "s_nop 0\n" \
  "s_or_b64 s[70:71], s[70:71], vcc\n" \
  "v_cmp_eq_u32 vcc, s26, v26\n" \
  "s_nop 0\n" \
  "s_or_b64 s[70:71], s[70:71], vcc\n" \
  "v_cmp_eq_u32 vcc, s26, v27\n" \
  "s_nop 0\n" \
  "s_or_b64 s[70:71], s[70:71], vcc\n" \
  "v_cmp_eq_u32 vcc, s26, v28\n" \
  "s_nop 0\n" \
  "s_or_b64 s[70:71], s[70:71], vcc\n" \
  "v_cmp_eq_u32 vcc, s26, v29\n" \
  "s_nop 0\n" \
  "s_or_b64 s[70:71], s[70:71], vcc\n" \
  "v_cmp_eq_u32 vcc, s26, v30\n" \
  "s_nop 0\n" \
  "s_or_b64 s[70:71], s[70:71], vcc\n" \
  "v_cmp_eq_u32 vcc, s26, v31\n" \
  "s_nop 0\n" \
  "s_or_b64 s[70:71], s[70:71], vcc\n" \
  "v_min_u32 v84, -2, v84\n" \
  "s_nop 7\n" \
  "s_cbranch_scc0 L_contb_%=\n" \
  "L_slowbr_%=:\n" \
  "s_sleep 2\n" \
  "s_memtime s[64:65]\n" \
  "s_waitcnt lgkmcnt(0)\n" \
  "s_mov_b32 s73, s64\n" \
  "s_add_u32 s72, s72, 1\n" \
  "s_and_b32 s70, s72, 0xffff\n" \
  "s_cmp_lt_u32 s70, 0xfff0\n" \
  "s_cbranch_scc1 L_spbw_%=\n" \
  "v_min_u32 v84, -2, v84\n" \
  "s_branch L_contb_%=\n" \
  "L_gchk_%=:\n" \
  "s_xor_b32 s68, s68, 0x10000\n" \
  "s_mov_b32 m0, s68\n" \
  "s_nop 0\n" \
  "buffer_load_dwordx4 v113, s[36:39], s35 offen sc1 lds\n" \
  "s_xor_b32 m0, m0, 0x1000\n" \
  "s_nop 0\n" \
  "buffer_load_dwordx4 v113, s[36:39], s40 offen sc1 lds\n" \
  "s_xor_b32 s68, s68, 0x10000\n" \
  "s_waitcnt lgkmcnt(0)\n" \
  "v_max_u32 v32, v0, v1\n" \
  "v_max3_u32 v32, v32, v2, v3\n" \
  "v_max3_u32 v32, v32, v4, v5\n" \
  "v_max3_u32 v32, v32, v6, v7\n" \
  "v_max3_u32 v32, v32, v8, v9\n" \
  "v_max3_u32 v32, v32, v10, v11\n" \
  "v_max3_u32 v32, v32, v12, v13\n" \
  "v_max3_u32 v32, v32, v14, v15\n" \
  "v_max3_u32 v32, v32, v16, v17\n" \
  "v_max3_u32 v32, v32, v18, v19\n" \
  "v_max3_u32 v32, v32, v20, v21\n" \
  "v_max3_u32 v32, v32, v22, v23\n" \
  "v_max3_u32 v32, v32, v24, v25\n" \
  "v_max3_u32 v32, v32, v26, v27\n" \
  "v_max3_u32 v32, v32, v28, v29\n" \
  "v_max3_u32 v32, v32, v30, v31\n" \
  "v_cmp_eq_u32 vcc, -1, v32\n" \
  "s_nop 3\n" \
  "s_cbranch_vccz L_conta_%=\n" \
  "s_add_u32 s27, s27, 1\n" \
  "s_cmp_lt_u32 s27, 0x10000\n" \
  "s_cbranch_scc0 L_conta_%=\n" \
  "v_mov_b32 v104, s26\n" \
  "buffer_store_dwordx2 v[104:105], v106, s[36:39], s32 offen sc1\n" \
  "s_xor_b32 s68, s68, 0x10000\n" \
  "v_xor_b32 v116, 0x10000, v116\n" \
  "v_xor_b32 v37, 0x10000, v37\n" \
  "v_xor_b32 v38, 0x10000, v38\n" \
  "v_xor_b32 v39, 0x10000, v39\n" \
  "v_mov_b64 v[84:85], 0\n" \
  "v_mov_b64 v[86:87], 0\n" \
  "v_mov_b64 v[88:89], 0\n" \
  "v_mov_b64 v[90:91], 0\n" \
  "v_mov_b64 v[92:93], 0\n" \
  "v_mov_b64 v[94:95], 0\n" \
  "v_mov_b64 v[96:97], 0\n" \
  "v_mov_b64 v[98:99], 0\n" \
  "s_waitcnt vmcnt(1)\n" \
  "s_branch L_body_%=\n" \
  "L_strep_%=:\n" \
  "s_nop 7\n" \
  "s_sleep 2\n" \
  "s_add_u32 s69, s69, 1\n" \
  "s_cmp_gt_u32 s69, 0x80000\n" \
  "s_cbranch_scc1 L_strd_%=\n" \
  "buffer_load_dwordx2 v[44:45], v125, s[36:39], s32 offen sc1\n" \
  "s_waitcnt vmcnt(0) lgkmcnt(0)\n" \
  "v_cmp_eq_u32 vcc, s26, v44\n" \
  "s_cbranch_vccnz L_strep_%=\n" \
  "L_strd_%=:\n" \
  "s_nop 7\n" \
  "v_add_u32 v44, 0x1, v44\n" \
  "s_mov_b32 s69, 0\n" \
  "s_branch L_stchk_%=\n"
#define REC_ASM_CLOBBER "v255", "a255", "s99", "vcc", "memory"

#define DLY_A 95
#define DLY_B 255
constexpr float SCL2 = 0.08838834764831845f * 1.4426950408889634f;
constexpr u32 OS = 144;
constexpr u32 L_O = 8192, L_TAB = 131072;
constexpr int LDS_BYTES = 163840;
DEV void role_rec(const Params& p, int b, int t, int lane, int w) {
  const int me = (b & 3) * 16 + (b >> 3);
  const int h = me >> 1, hf = me & 1;
  const int c = lane & 15, g = lane >> 4, u = (lane >> 3) & 1;
  {
    u32 tb[31];
#pragma unroll
    for (int j = 0; j < 4; ++j) { const int sg = (4 * w + j) * 4 + (lane >> 4); tb[20 + j] = rowmap<MAP_RQKV>(sg) * 8192 + ((((lane >> 1) & 7) ^ (sg & 7)) * 16 + (lane & 1) * 8) * 2; }
#pragma unroll 1
    for (int i = 0; i < 8; ++i) {
      const char* src = (const char*)(p.wq + h * DH) + (size_t)(i ^ (4 * hf)) * 64 * 8192;
#pragma unroll
      for (int j = 0; j < 4; ++j) glds16(src + tb[20 + j], i * 16384 + w * 4096 + j * 1024);
    }
    const u32 xr = 4 * g + ((lane >> 2) & 3);
    tb[0] = (lane & 2) ? 0xEEEE : 0x4444;
    tb[1] = lane * 16; tb[2] = 12288 + lane * 16 + w * 4; tb[3] = 2048 + (32 * w + c) * 32 + (((g + 3) & 3) ^ (c >> 2)) * 8 + 4;
    tb[4] = w * 1024 + (g >> 1) * 512 + ((lane >> 2) & 3) * 128 + (g & 1) * 64 + (lane & 3) * 16;
    tb[5] = ((u32)(2 * w) << 18) + ((u32)(u * 4 + g + 1) << 14) + me * 128 + (lane & 7) * 16;
    tb[6] = u ? 0x40000000u : me * 128 + g * 32 + (lane & 7) * 4;
    tb[7] = (32 * w + c) * 16 + g * 4;
    tb[8] = 2048 + (32 * w + c) * 32 + (g ^ (c >> 2)) * 8;
    tb[9] = xr * 16 + (lane & 1) * 8;
    tb[10] = 2048 + xr * 32 + ((lane & 3) ^ g) * 8;
    tb[11] = tb[10] + (64 * ((w - 1) & 1) + 16 * hf) * 32;
    tb[12] = 8192 + w * 1024 + lane * 16;
    tb[13] = (lane >> 2) * 128 + (lane & 3) * 8;
    tb[14] = L_O + c * OS + (16 * w + 4 * g) * 2;
    tb[15] = L_O + (2 * ((lane >> 2) & 3) + (lane & 1)) * OS + g * 16;
#pragma unroll
    for (int j = 0; j < 4; ++j) tb[16 + j] = c == j ? 0x00003F80u : (c == 4 + j ? 0x3F800000u : 0u);
#pragma unroll
    for (int j = 0; j < 2; ++j) { const int sg = (2 * w + j) * 8 + (lane >> 3); tb[24 + j] = rowmap<MAP_RQKV>(sg) * 8192 + ((((lane >> 1) & 3) ^ ((sg >> 1) & 3)) * 16 + (lane & 1) * 8) * 2; }
#pragma unroll
    for (int j = 0; j < 2; ++j) { const int q = lane >> 4; tb[26 + j] = q * 1024 + ((((lane >> 1) & 7) ^ (4 * j + 2 * (q & 1))) * 16 + (lane & 1) * 8) * 2; }
    const int sl = lane >> 2;
    tb[28] = w * 16384 + sl * 256 + (((2 * w) ^ (sl & 7)) * 32) + (lane & 3) * 8;
    tb[29] = hf * 32768 + w * 8192 + sl * 128 + ((w ^ ((sl >> 1) & 3)) * 32) + (lane & 3) * 8;
    tb[30] = 65536 + w * 16384 + (4 * g + (sl & 1)) * 256 + ((g & 1) * 4 + (sl & 1) * 2 + ((sl >> 1) & 1)) * 32 + (lane & 3) * 8;
#pragma unroll
    for (int j = 0; j < 31; ++j) lds_wr32(L_TAB + j * 1024 + t * 4, tb[j]);
  }
  {
    const u64 pw = (u64)p.ws, a0 = (u64)(p.wk + h * DH) + ((u64)hf << 21), a1 = (u64)(p.wk + h * DH) + ((u64)(hf ^ 1) << 21), a2 = (u64)(p.wv + h * DH + hf * 64),
              a3 = (u64)(p.wo + (size_t)(h * DH + hf * 64) * DM + w * 128);
    const u32 kv0 = w < 3 ? WS_UA + h * 16384 + (w - 1) * 4096 : WS_UB + h * 16384 + hf * 4096;
    const u32 km0 = w == 0 ? 0x40000000u : WS_UC + h * 16384 + (w < 3 ? hf * 2048 + (w - 1) * 1024 : 4096 + hf * 1024), vm0 = w * 1024 + hf * 4096;
    const u32 sc[25] = {(u32)a0, (u32)(a0 >> 32), (u32)a1, (u32)(a1 >> 32), (u32)a2, (u32)(a2 >> 32), (u32)a3, (u32)(a3 >> 32), (u32)pw, (u32)(pw >> 32) & 0xffffu, WS_END,
                        chunk_off(me), kv0, km0, vm0, (u32)p.dlya, (u32)p.dlyb, WS_DBG + (me * 4 + w) * 4096, 0u,
                        (u32)(b >> 3) * 128 + (b & 3) * 8, CT_FKV + h * 256, 2200u, 1200u, CT_FM + (me >> 4) * 16384 + ((me & 15) * 4 + w) * 128, CT_EPOCH};
    if (lane == 0) {
#pragma unroll
      for (int j = 0; j < 25; ++j) lds_wr32(L_TAB + 31 * 1024 + w * 256 + j * 4, sc[j]);
    }
  }
  asm volatile(REC_ASM_TEXT : : "s"(w) : REC_ASM_CLOBBER);
}

DEV void ln_load(const Params& p, int seg, int lane, int w, u32x4 (&xv)[8]) {
#pragma unroll
  for (int i = 0; i < 8; ++i) xv[i] = *(const u32x4*)(p.x + (size_t)(seg * 32 + (w & 1) * 16 + (lane & 15)) * DM + ((w >> 1) * 8 + i) * 32 + (lane >> 4) * 8);
}
DEV void ln_norm(u32x4 (&xv)[8], u32 sc, int lane, int w) {
  const int c = lane & 15, g = lane >> 4, rt = w & 1;
  float sum = 0;
#pragma unroll
  for (int i = 0; i < 8; ++i)
#pragma unroll
    for (int j = 0; j < 8; ++j) sum += bfel(xv[i], j);
  sum = xsum32(xsum16(sum));
  if (g == 0) lds_wr32(sc + w * 64 + c * 4, __float_as_uint(sum));
  bar();
  const float mean = (__uint_as_float(lds_rd32(sc + rt * 64 + c * 4)) + __uint_as_float(lds_rd32(sc + 128 + rt * 64 + c * 4))) * (1.f / 512.f);
  float sq = 0;
#pragma unroll
  for (int i = 0; i < 8; ++i)
#pragma unroll
    for (int j = 0; j < 8; ++j) { const float d = bfel(xv[i], j) - mean; sq = fmaf(d, d, sq); }
  sq = xsum32(xsum16(sq));
  if (g == 0) lds_wr32(sc + 256 + w * 64 + c * 4, __float_as_uint(sq));
  bar();
  sq = __uint_as_float(lds_rd32(sc + 256 + rt * 64 + c * 4)) + __uint_as_float(lds_rd32(sc + 384 + rt * 64 + c * 4));
  const float mb = bfr(mean), rs = __builtin_amdgcn_rcpf(bfr(__builtin_amdgcn_sqrtf(bfr(bfr(sq * (1.f / 512.f)) + 1e-5f))));
#pragma unroll
  for (int i = 0; i < 8; ++i)
#pragma unroll
    for (int j = 0; j < 4; ++j) {
      const u32 d = pk2(bflo(xv[i][j]) - mb, bfhi(xv[i][j]) - mb);
      xv[i][j] = pk2(bflo(d) * rs, bfhi(d) * rs);
    }
}
DEV u32x4 ln_mod(u32x4 n, u32 md, int i, int lane, int w) {
  const u32x4 sa = lds_rd128(md + ((w >> 1) * 8 + i) * 64 + (lane >> 4) * 16), ca = lds_rd128(md + 1024 + ((w >> 1) * 8 + i) * 64 + (lane >> 4) * 16);
  u32x4 o;
#pragma unroll
  for (int j = 0; j < 4; ++j) {
    const u32 m = pk2(bflo(n[j]) * bflo(ca[j]), bfhi(n[j]) * bfhi(ca[j]));
    o[j] = pk2(bflo(m) + bflo(sa[j]), bfhi(m) + bfhi(sa[j]));
  }
  return o;
}
template <int MODE, int NIT> DEV u32 bg_proj(const Params& p, u32 tag, rsrc_t rw, const u16* w0, const u16* w1, int h, int s0, int stride, u32 cline, int t, int lane, int w) {
  u32x8 W[2][2][8];
  const int x = (MODE == 0 ? s0 : 0) * 4;
  u32x4 hv[8];
  u32 par = 0;
#pragma unroll
  for (int rd = 0; rd < 2; ++rd)
#pragma unroll
    for (int i = 0; i < 4; ++i) slab_dma<8, MAP_NAT, 4>(w0 + (size_t)((rd * 4 + i) ^ x) * 64 * 4096, 4096, rd * 65536 + i * 16384, w * 4, lane);
#pragma unroll
  for (int rd = 0; rd < 4; ++rd) {
    if (rd < 3) VMWAIT(16);
    else {
      par = wait_start<true>(rw, tag, lane, MODE == 0 ? 7 : 63);
      VMWAIT(0);
      TS(15);
#pragma unroll
      for (int i = 0; i < 8; ++i) hv[i] = ld128(rw, t * 16 + (i & 1) * 4096, WS_H + s0 * 32768 + (((i >> 1) * 8192) ^ (x * 2048)));
    }
    bar();
#pragma unroll
    for (int i = 0; i < 4; ++i)
#pragma unroll
      for (int tl = 0; tl < 2; ++tl) W[rd >> 1][tl][(rd & 1) * 4 + i] = slab_frag<8>((rd & 1) * 65536 + i * 16384, 2 * w + tl, lane);
    if (rd < 2) {
      bar();
#pragma unroll
      for (int i = 0; i < 4; ++i) slab_dma<8, MAP_NAT, 4>(w1 + (size_t)((rd * 4 + i) ^ x) * 64 * 4096, 4096, rd * 65536 + i * 16384, w * 4, lane);
    }
  }
  TS(1);
#pragma unroll
  for (int i = 0; i < 8; ++i) lds_wr128(t * 16 + i * 4096, hv[i]);
  TS(2);
  bar();
#pragma unroll 1
  for (int it = 0; it < NIT; ++it) {
    const int s = s0 + stride * it, sn = it + 1 < NIT ? s + stride : s;
    LAS char* lb = (LAS char*)(uintptr_t)(lane * 16 + (it & 1) * 32768);
    if (MODE == 0 && it == 3) wait_start<true>(rw, tag, lane);
    const char* hsrc = p.ws + WS_H + sn * 32768 + ((w * 8192) ^ (x * 2048)) + lane * 16;
    const u32 hdst = ((it & 1) ^ 1) * 32768 + w * 8192;
    f32x4 acc[4][2];
    u32x4 hf[4];
#pragma unroll
    for (int n = 0; n < 3; ++n) hf[n] = lds_rd128o(lb, ((n & 1) * 16 + (n >> 1)) * 1024);
    SCHED();
#pragma unroll
    for (int n = 0; n < 32; ++n) {
      const int ks = n >> 1, rt = n & 1;
      if (n + 3 < 32) hf[(n + 3) & 3] = lds_rd128o(lb, (((n + 3) & 1) * 16 + ((n + 3) >> 1)) * 1024);
      if (n >= 6 && n < 22 && !(n & 1)) glds16c(hsrc + ((n - 6) >> 1) * 1024, hdst + ((n - 6) >> 1) * 1024);
#pragma unroll
      for (int i = 0; i < 4; ++i) {
        const u32x4 wf = half4(W[i >> 1][i & 1][ks >> 1], ks & 1);
        const f32x4 ci = ks ? acc[i][rt] : (f32x4){0, 0, 0, 0};
        acc[i][rt] = (MODE == 1 || i < 2) ? mfma32(wf, hf[n & 3], ci) : mfma32(hf[n & 3], wf, ci);
      }
      if (n == 11) {
        VMWAIT(3);
        __builtin_amdgcn_s_barrier();
        if (t == 0 && it > 1) st32(it, rw, 0, cline);
      }
      SCHED();
    }
    VMWAIT(0);
    const u32 ob = (u32)(s * 32 + h) * 16384 + (MODE == 0 ? nearh(par, s >> 2) : farh(par, s >> 2));
    if (MODE == 0) {
#pragma unroll
      for (int rt = 0; rt < 2; ++rt) st128(pk8(acc[0][rt], acc[1][rt]), rw, lane * 16, WS_UA + ob + (rt * 4 + w) * 1024);
#pragma unroll
      for (int i = 0; i < 2; ++i) st128(pk8(acc[2 + i][0], acc[2 + i][1]), rw, lane * 16, WS_UB + ob + (2 * w + i) * 1024);
      if (s >= 61) {
#pragma unroll
        for (int i = 0; i < 6; ++i) st128((u32x4){SENT, SENT, SENT, SENT}, rw, t * 16 + (i & 1) * 4096, i < 2 ? WS_UB + (ob ^ 8192u) : WS_UC + (ob ^ ((i >> 1) & 1) * 8192u));
      }
    } else {
#pragma unroll
      for (int m = 0; m < 2; ++m)
#pragma unroll
        for (int rt = 0; rt < 2; ++rt) st128(pk8(acc[2 * m][rt], acc[2 * m + 1][rt]), rw, lane * 16, WS_UA + ob + m * 16384 + (rt * 4 + w) * 1024);
    }
    if (it == 0) VMWAIT(0);
    bar();
    if (it == 0 && t == 0) st32(1u, rw, 0, cline);
    if (it == 0) TS(3);
  }
  VMWAIT(0);
  bar();
  if (t == 0) st32(NIT, rw, 0, cline);
  TS(4);
  return par;
}
DEV u64 rkey(u32 bits, int idx) {
  const float f = __uint_as_float(bits);
  const u32 o = (f != f) ? 0u : ((bits & 0x80000000u) ? ~bits : (bits | 0x80000000u));
  return ((u64)o << 32) | (u32)(0x7FFFFFFF - idx);
}
DEV void top2_push(u64& k0, u64& k1, u64 k) { const u64 lo = k < k0 ? k : k0; k0 = k < k0 ? k0 : k; k1 = lo > k1 ? lo : k1; }
DEV u64 shx(u64 v, int m) { return ((u64)(u32)__shfl_xor((int)(v >> 32), m) << 32) | (u32)__shfl_xor((int)(u32)v, m); }
DEV int att_seg(int r, int it) { return (r < 3 ? (it < 8 ? r + 3 * it : r + 6 * it - 18) : (it < 2 ? 21 + r + 3 * it : 18 + r + 6 * it)); }
constexpr int SPEC_CUT = 59;
DEV void bg_att(const Params& p, u32 tag, rsrc_t rw, int hp, int r, int t, int lane, int w) {
  u32x8 W[4][8];
  {
    const u16* so = p.wo + (size_t)(hp * 256) * DM + w * 128;
    slab_dma<8, MAP_ACC, 16>(so, DM, w * 32768, 0, lane);
    slab_dma<8, MAP_ACC, 16>(so + 64 * DM, DM, w * 32768 + 16384, 0, lane);
#pragma unroll
    for (int rd = 0; rd < 4; ++rd) {
      if (rd < 3) VMWAIT(16); else VMWAIT(0);
#pragma unroll
      for (int i = 0; i < 8; ++i) W[rd][i] = slab_frag<8>(w * 32768 + (rd & 1) * 16384, i, lane);
      asm volatile("s_waitcnt lgkmcnt(0)" ::: "memory");
      if (rd < 2) slab_dma<8, MAP_ACC, 16>(so + (size_t)(rd + 2) * 64 * DM, DM, w * 32768 + (rd & 1) * 16384, 0, lane);
    }
  }
  bar();
  const u32 par = wait_start<false>(rw, tag, lane);
  TS(9);
  constexpr u32 L_OF = 0, L_OUT = 16384, OUTS = 272, L_FIN = 53248, L_MV = 61440;
  const int c = lane & 15, g = lane >> 4, h = hp * 2 + (w >> 1), qt = w & 1, nit = r > 3 ? 7 : (r == 3 ? 8 : 14);
  LAS char* lb = (LAS char*)(uintptr_t)(lane * 16);
  LAS char* ow = (LAS char*)(uintptr_t)(L_OUT + w * 32 * OUTS + c * OUTS + g * 8);
  LAS char* orr = (LAS char*)(uintptr_t)(L_OUT + w * 32 * OUTS + g * OUTS + c * 16);
  const rsrc_t ro = mkrsrc(p.out, NROW * DM * 2);
  u64 k0 = 0, k1 = 0;
  u32x4 qf[4], vx[8];
  float e[10], mx;
  constexpr u32 L_ST = 67584;
  u32 have = 0;
#define ATT_CHECK(S_) do { \
    const u32 need_ = (lane & 1) ? (S_) + 1 : ((((lane >> 1) ^ (S_)) & 1) ? 0 : ((S_) >> 1) + 1); \
    for (u32 sp = 0; sp < SPINCAP && !__all(have >= need_); ++sp) { \
      asm volatile("" ::: "memory"); \
      if (sp) __builtin_amdgcn_s_sleep(POLLSLEEP); \
      have = ld32(rw, (lane & 1) ? CT_FQ + hp * 128 : CT_FKV + (h * 2 + ((lane >> 1) & 1)) * 128, 0); \
    } \
    asm volatile("" ::: "memory"); \
  } while (0)
#define ATT_PIECE(UN_, N_) glds16c(p.ws + ((N_) < 4 ? WS_UA + ((UN_) ^ 8192u) : ((N_) < 8 ? WS_UA + (UN_) : WS_UB + (UN_))) + qt * 4096 + ((N_) & 3) * 1024 + lane * 16, \
                                   L_ST + (w >> 1) * 24576 + ((N_) >> 2) * 8192 + qt * 4096 + ((N_) & 3) * 1024)
#define ATT_PRE() do { \
    LAS char* sb_ = lb + L_ST + (w >> 1) * 24576; \
    f32x4 s0 = {0, 0, 0, 0}, s1 = s0; \
    _Pragma("unroll") for (int ks = 0; ks < 4; ++ks) { \
      qf[ks] = lds_rd128o(sb_ + qt * 4096, ks * 1024); \
      s0 = mfma32(lds_rd128o(sb_, 8192 + ks * 1024), qf[ks], s0); s1 = mfma32(lds_rd128o(sb_, 12288 + ks * 1024), qf[ks], s1); \
    } \
    _Pragma("unroll") for (int i = 0; i < 8; ++i) vx[i] = lds_rd128o(sb_, 16384 + i * 1024); \
    const float NEG = -__builtin_inff(); \
    _Pragma("unroll") for (int i = 0; i < 4; ++i) { const bool vis = 4 * g + i <= c; e[i] = (qt || vis) ? s0[i] : NEG; e[4 + i] = (qt && vis) ? s1[i] : NEG; } \
    mx = e[0]; \
    _Pragma("unroll") for (int i = 1; i < 8; ++i) mx = fmaxf(mx, e[i]); \
  } while (0)
  mod_gather<2>(rw, tag, 4096, L_MV, t);
  if (w == 0) *(LAS u32x4*)(lb + L_MV + 4096) = *(const u32x4*)(p.wr + lane * 8);
  {
    const int s = att_seg(r, 0);
    const u32 un = (u32)(s * 32 + h) * 16384 + nearh(par, s >> 2);
    ATT_CHECK(s);
#pragma unroll
    for (int n = 0; n < 12; ++n) ATT_PIECE(un, n);
    VMWAIT(0);
    bar();
    ATT_PRE();
  }
#pragma unroll 1
  for (int it = 0; it < nit; ++it) {
    const int s = att_seg(r, it);
    const u32 un = (u32)(s * 32 + h) * 16384 + nearh(par, s >> 2), opb = (u32)s * 524288 + farh(par, s >> 2);
    const int rl = 2 * hp + (w >> 1), row = s * 32 + rl;
    const u32x4 xv = *(const u32x4*)(p.x + (size_t)row * DM + lane * 8);
    const bool last = s >= 62, more = it + 1 < nit;
    const int sn = more ? att_seg(r, it + 1) : s;
    const u32 unn = (u32)(sn * 32 + h) * 16384 + nearh(par, sn >> 2);
    ATT_CHECK(sn);
    wait_fm(rw, par, h, s < 61 ? s + 1 : min(s, 62), lane);
    if (s == 63) __builtin_amdgcn_s_sleep(GOSLEEP);
    TS(10);
    u32x4 vm[2];
    f32x4 sm;
    for (u32 sp = 0; sp < SPINCAP; ++sp) {
      asm volatile("" ::: "memory");
      u32x4 ka[2], kb[2];
#pragma unroll
      for (int i = 0; i < 2; ++i) { ka[i] = ld128(rw, lane * 16 + i * 1024, WS_UC + un); kb[i] = ld128(rw, lane * 16 + i * 1024 + 2048, WS_UC + un); vm[i] = ld128(rw, lane * 16 + i * 1024, WS_UC + un + 4096); }
      sm = (f32x4){0, 0, 0, 0};
#pragma unroll
      for (int ks = 0; ks < 4; ++ks) sm = mfma32((u32x4){ka[ks >> 1][(ks & 1) * 2], ka[ks >> 1][(ks & 1) * 2 + 1], kb[ks >> 1][(ks & 1) * 2], kb[ks >> 1][(ks & 1) * 2 + 1]}, qf[ks], sm);
      if (s < 61 || !__any(hassent(ka[0]) || hassent(ka[1]) || hassent(kb[0]) || hassent(kb[1]) || hassent(vm[0]) || hassent(vm[1]))) break;
      __builtin_amdgcn_s_sleep(2);
    }
    e[8] = sm[0]; e[9] = sm[1];
    TSV(1, e[8]);
    mx = fmaxf(mx, fmaxf(e[8], e[9]));
    mx = xmax32(xmax16(mx));
    const float nm = -mx * SCL2;
#pragma unroll
    for (int i = 0; i < 10; ++i) e[i] = __builtin_amdgcn_exp2f(fmaf(e[i], SCL2, nm));
    const u32x4 pb = {pk2(e[0], e[1]), pk2(e[2], e[3]), pk2(e[4], e[5]), pk2(e[6], e[7])};
    const u32x4 pm = {pk2(e[8], e[9]), 0u, 0u, 0u};
    float ls = bflo(pm[0]) + bfhi(pm[0]);
#pragma unroll
    for (int i = 0; i < 4; ++i) ls += bflo(pb[i]) + bfhi(pb[i]);
    ls = xsum32(xsum16(ls));
    const float inv = __builtin_amdgcn_rcpf(ls);
#pragma unroll
    for (int ks = 0; ks < 4; ++ks) {
      f32x4 oa = mfma32(vx[2 * ks], pb, (f32x4){0, 0, 0, 0}); oa = mfma32((u32x4){vm[ks >> 1][(2 * ks) & 3], 0u, 0u, 0u}, pm, oa);
      f32x4 ob = mfma32(vx[2 * ks + 1], pb, (f32x4){0, 0, 0, 0}); ob = mfma32((u32x4){vm[ks >> 1][(2 * ks + 1) & 3], 0u, 0u, 0u}, pm, ob);
      *(LAS u32x4*)(lb + L_OF + (qt * 8 + (w >> 1) * 4 + ks) * 1024) = pk8(oa * inv, ob * inv);
    }
    bar();
    TS(2);
    {
      const u32 od = (hp < 8 ? WS_UB : WS_UC) + opb + (hp & 7) * 65536 + g * 1024 + w * 256 + c * 16;
      u32x4 of[3], tr[4];
      of[0] = lds_rd128o(lb, L_OF); of[1] = lds_rd128o(lb, L_OF + 1024);
      f32x4 acc[8];
      SCHED();
#pragma unroll
      for (int n = 0; n < 16; ++n) {
        const int ks = n & 7, rt = n >> 3;
        if (n + 2 < 16) of[(n + 2) % 3] = lds_rd128o(lb, L_OF + (n + 2) * 1024);
        if (n < 8 && more) { ATT_PIECE(unn, n); if (n < 4) ATT_PIECE(unn, 8 + n); }
        if (n == 9) {
#pragma unroll
          for (int i = 0; i < 4; ++i) tr[i] = lds_rd128o(orr, i * 4 * OUTS);
        }
        if (n == 11) {
#pragma unroll
          for (int i = 0; i < 4; ++i) st128(tr[i], rw, od + (i & 1) * 4096 + (i >> 1) * 16384, 0);
        }
#pragma unroll
        for (int i = 0; i < 8; ++i) acc[i] = mfma32(half4(W[ks >> 1][i], ks & 1), of[n % 3], ks ? acc[i] : (f32x4){0, 0, 0, 0});
        if (ks == 7) {
#pragma unroll
          for (int i = 0; i < 8; ++i) *(LAS u32x2*)(ow + rt * 16 * OUTS + i * 32) = (u32x2){pk2(acc[i][0], acc[i][1]), pk2(acc[i][2], acc[i][3])};
        }
        SCHED();
      }
#pragma unroll
      for (int i = 0; i < 4; ++i) tr[i] = lds_rd128o(orr, (16 + i * 4) * OUTS);
#pragma unroll
      for (int i = 0; i < 4; ++i) st128(tr[i], rw, od + 32768 + (i & 1) * 4096 + (i >> 1) * 16384, 0);
    }
    TS(3);
    if (!last) {
      VMWAIT(0);
      bar();
      TS(4);
      if (t == 0) st32(it + 1, rw, 0, CT_FO + (r * 16 + hp) * 128);
      if (more) ATT_PRE();
      for (u32 sp = 0; sp < SPINCAP; ++sp) {
        asm volatile("" ::: "memory");
        __builtin_amdgcn_s_sleep(FOSLEEP);
        const u32 v = ld32(rw, c * 128, CT_FO + r * 2048);
        if (__all(v >= (u32)(it + 1))) break;
      }
    } else __builtin_amdgcn_s_sleep(LASTSLEEP);
    TS(6);
    float at[8];
    for (u32 sp = 0; sp < SPINCAP; ++sp) {
      asm volatile("" ::: "memory");
      u32x4 pv[8];
#pragma unroll
      for (int i = 0; i < 8; ++i) pv[i] = ld128(rw, lane * 16, (qt ? WS_UC : WS_UB) + opb + i * 65536 + (rl >> 3) * 16384 + (rl & 7) * 1024);
#pragma unroll
      for (int j = 0; j < 8; ++j) at[j] = 0.f;
#pragma unroll
      for (int i = 0; i < 8; ++i)
#pragma unroll
        for (int j = 0; j < 8; ++j) at[j] += bfel(pv[i], j);
      if (!last) break;
      const float chk = (at[0] + at[2]) + (at[4] + at[6]);
      if (!__any(chk != chk)) break;
      bool bad = false;
#pragma unroll
      for (int i = 0; i < 8; ++i) bad = bad || hassent(pv[i]);
      if (!__any(bad)) break;
      __builtin_amdgcn_s_sleep(2);
    }
    if (qt) {
      *(LAS f32x4*)(lb + L_FIN + (w >> 1) * 2048) = (f32x4){at[0], at[1], at[2], at[3]};
      *(LAS f32x4*)(lb + L_FIN + (w >> 1) * 2048 + 1024) = (f32x4){at[4], at[5], at[6], at[7]};
    }
    bar();
    TS(7);
    if (!qt) {
      const f32x4 o0 = *(LAS f32x4*)(lb + L_FIN + (w >> 1) * 2048), o1 = *(LAS f32x4*)(lb + L_FIN + (w >> 1) * 2048 + 1024);
      const u32x4 ga = lds_rd128o(lb, L_MV), sb = lds_rd128o(lb, L_MV + 1024), cb = lds_rd128o(lb, L_MV + 2048), gb = lds_rd128o(lb, L_MV + 3072), wr = lds_rd128o(lb, L_MV + 4096);
      float x1[8], h2[8], o[8];
#pragma unroll
      for (int j = 0; j < 4; ++j) {
        const u32 a2 = pk2(at[2 * j] + (j < 2 ? o0[(2 * j) & 3] : o1[(2 * j) & 3]), at[2 * j + 1] + (j < 2 ? o0[(2 * j + 1) & 3] : o1[(2 * j + 1) & 3]));
        const u32 g2 = pk2(bflo(ga[j]) * bflo(a2), bfhi(ga[j]) * bfhi(a2));
        const u32 x2 = pk2(bflo(xv[j]) + bflo(g2), bfhi(xv[j]) + bfhi(g2));
        x1[2 * j] = bflo(x2); x1[2 * j + 1] = bfhi(x2);
      }
      const float piv = __uint_as_float(__builtin_amdgcn_readfirstlane(__float_as_uint(x1[0])));
      float sum = 0, sq = 0;
#pragma unroll
      for (int j = 0; j < 8; ++j) { const float d = x1[j] - piv; sum += d; sq = fmaf(d, d, sq); }
      sum = __uint_as_float(__builtin_amdgcn_readfirstlane(__float_as_uint(wave_sum(sum)))) * (1.f / 512.f);
      sq = __uint_as_float(__builtin_amdgcn_readfirstlane(__float_as_uint(wave_sum(sq)))) * (1.f / 512.f);
      const float mb = bfr(piv + sum), rs = __builtin_amdgcn_rcpf(bfr(__builtin_amdgcn_sqrtf(bfr(bfr(fmaxf(fmaf(-sum, sum, sq), 0.f)) + 1e-5f))));
      float rr = 0;
#pragma unroll
      for (int j = 0; j < 4; ++j) {
        const u32 d2 = pk2(x1[2 * j] - mb, x1[2 * j + 1] - mb);
        const u32 n2 = pk2(bflo(d2) * rs, bfhi(d2) * rs);
        const u32 m2 = pk2(bflo(n2) * bflo(cb[j]), bfhi(n2) * bfhi(cb[j]));
        const u32 hh = pk2(bflo(m2) + bflo(sb[j]), bfhi(m2) + bfhi(sb[j]));
        h2[2 * j] = bflo(hh); h2[2 * j + 1] = bfhi(hh);
        rr = fmaf(h2[2 * j], bflo(wr[j]), fmaf(h2[2 * j + 1], bfhi(wr[j]), rr));
        const u32 t2 = pk2(bflo(gb[j]) * h2[2 * j], bfhi(gb[j]) * h2[2 * j + 1]);
        o[2 * j] = x1[2 * j] + bflo(t2); o[2 * j + 1] = x1[2 * j + 1] + bfhi(t2);
      }
      st128((u32x4){pk2(h2[0], h2[1]), pk2(h2[2], h2[3]), pk2(h2[4], h2[5]), pk2(h2[6], h2[7])}, rw, lane * 16, WS_H2 + rowoff(par, row));
      st128((u32x4){pk2(x1[0], x1[1]), pk2(x1[2], x1[3]), pk2(x1[4], x1[5]), pk2(x1[6], x1[7])}, rw, lane * 16, WS_X1 + rowoff(par, row));
      st128((u32x4){pk2(o[0], o[1]), pk2(o[2], o[3]), pk2(o[4], o[5]), pk2(o[6], o[7])}, ro, lane * 16, row * 1024);
      top2_push(k0, k1, rkey(__builtin_amdgcn_readfirstlane(__float_as_uint(wave_sum(rr))), row));
    }
    const bool rec0 = s <= SPEC_CUT && (!more || sn > SPEC_CUT);
    if (rec0 || !more) {
      if (lane == 0 && w == 2) lds_wr128(L_FIN + 4096, (u32x4){(u32)k0, (u32)(k0 >> 32), (u32)k1, (u32)(k1 >> 32)});
      VMWAIT(0);
      bar();
      if (w == 0) {
        const u32x4 v = lds_rd128(L_FIN + 4096);
        u64 m0 = k0, m1 = k1;
        top2_push(m0, m1, ((u64)v[1] << 32) | v[0]); top2_push(m0, m1, ((u64)v[3] << 32) | v[2]);
        if (lane < 2 && (lane ? !more : rec0)) st128((u32x4){(u32)m0, (u32)(m0 >> 32), (u32)m1, (u32)(m1 >> 32)}, rw, lane * 16384, CT_TOP + (r * 16 + hp) * 128);
      }
    }
  }
  TS(11);
}

template <int NC> DEV void mod_slice(const Params& p, u32 tag, rsrc_t rw, int col0, int t, int lane, int w, u32x4 (&xv)[8], int xa) {
  constexpr int NR = 2 * NC;
  const int cg = t & (NC - 1), kg = t / NC;
  u32x4 wv[NR];
#pragma unroll
  for (int i = 0; i < NR; ++i) wv[i] = *(const u32x4*)(p.wmod + (size_t)(kg * NR + i) * 3072 + col0 + cg * 8);
  u32 cv2[NC];
#pragma unroll
  for (int i = 0; i < NC; ++i) cv2[i] = *(const u32*)(p.c + kg * NR + 2 * i);
  if (xa >= 0) ln_load(p, xa, lane, w, xv);
  float acc[8] = {0, 0, 0, 0, 0, 0, 0, 0};
#pragma unroll
  for (int i = 0; i < NR; ++i) {
    const float cv = (i & 1) ? bfhi(cv2[i >> 1]) : bflo(cv2[i >> 1]), sv = bfr(cv / (1.f + __expf(-cv)));
#pragma unroll
    for (int j = 0; j < 8; ++j) acc[j] += sv * bfel(wv[i], j);
  }
#pragma unroll
  for (int j = 0; j < 8; ++j) {
    if (NC <= 2) acc[j] += dppf<0x122>(acc[j]);
    acc[j] += dppf<0x124>(acc[j]); acc[j] += dppf<0x128>(acc[j]); acc[j] = xsum32(xsum16(acc[j]));
  }
  if (lane < NC) { *(LAS f32x4*)(uintptr_t)(1024 + w * 256 + cg * 32) = (f32x4){acc[0], acc[1], acc[2], acc[3]}; *(LAS f32x4*)(uintptr_t)(1024 + w * 256 + cg * 32 + 16) = (f32x4){acc[4], acc[5], acc[6], acc[7]}; }
  bar();
  if (t < NC * 4) {
    float m0 = 0, m1 = 0;
#pragma unroll
    for (int k = 0; k < 4; ++k) { const u32x2 v = lds_rd64(1024 + k * 256 + t * 8); m0 += __uint_as_float(v[0]); m1 += __uint_as_float(v[1]); }
    m0 = bfr(m0); m1 = bfr(m1);
    const int which = col0 >> 9;
    if (which == 1 || which == 4) { m0 += 1.f; m1 += 1.f; }
    st64((u32x2){pk2(m0, m1), mktag(tag)}, rw, t * 8, WS_MOD + col0 * 4);
  }
}
DEV void role_aux(const Params& p, u32 tag, rsrc_t rw, int a, int t, int lane, int w) {
  u32x4 xv[8];
  {
    const int cg = t & 7, kr = (a >> 4) * 128 + (t >> 3) * 4;
    u32x4 wv[4];
#pragma unroll
    for (int i = 0; i < 4; ++i) wv[i] = *(const u32x4*)(p.wmod + (size_t)(kr + i) * 3072 + (a & 15) * 64 + cg * 8);
    const u32x2 cv2 = *(const u32x2*)(p.c + kr);
    ln_load(p, a, lane, w, xv);
    float acc[8] = {0, 0, 0, 0, 0, 0, 0, 0};
#pragma unroll
    for (int i = 0; i < 4; ++i) {
      const float cv = (i & 1) ? bfhi(cv2[i >> 1]) : bflo(cv2[i >> 1]), sv = bfr(cv / (1.f + __expf(-cv)));
#pragma unroll
      for (int j = 0; j < 8; ++j) acc[j] += sv * bfel(wv[i], j);
    }
#pragma unroll
    for (int j = 0; j < 8; ++j) { acc[j] += dppf<0x128>(acc[j]); acc[j] = xsum32(xsum16(acc[j])); }
    if (lane < 8) { *(LAS f32x4*)(uintptr_t)(1024 + w * 256 + cg * 32) = (f32x4){acc[0], acc[1], acc[2], acc[3]}; *(LAS f32x4*)(uintptr_t)(1024 + w * 256 + cg * 32 + 16) = (f32x4){acc[4], acc[5], acc[6], acc[7]}; }
    bar();
    if (t < 64) {
      const float m = (__uint_as_float(lds_rd32(1024 + t * 4)) + __uint_as_float(lds_rd32(1280 + t * 4))) + (__uint_as_float(lds_rd32(1536 + t * 4)) + __uint_as_float(lds_rd32(1792 + t * 4)));
      st64((u32x2){__float_as_uint(m), mktag(tag)}, rw, t * 8, WS_MODP + a * 512);
    }
  }
  ln_norm(xv, 0, lane, w);
  TSV(12, xv[7][3]);
  {
    const u32 tg = mktag(tag);
    u32x4 v[8];
    for (u32 sp = 0; sp < SPINCAP; ++sp) {
      asm volatile("" ::: "memory");
      bool ok = true;
#pragma unroll
      for (int i = 0; i < 8; ++i) v[i] = ld128(rw, t * 16 + (i & 1) * 4096, WS_MODP + (i >> 1) * 8192);
#pragma unroll
      for (int i = 0; i < 8; ++i) ok = ok && v[i][1] == tg && v[i][3] == tg;
      if (__all(ok)) break;
      __builtin_amdgcn_s_sleep(4);
    }
#pragma unroll
    for (int k = 0; k < 2; ++k) {
      const float m0 = bfr((__uint_as_float(v[k][0]) + __uint_as_float(v[2 + k][0])) + (__uint_as_float(v[4 + k][0]) + __uint_as_float(v[6 + k][0])));
      const float m1 = bfr((__uint_as_float(v[k][2]) + __uint_as_float(v[2 + k][2])) + (__uint_as_float(v[4 + k][2]) + __uint_as_float(v[6 + k][2])));
      lds_wr32(4096 + k * 1024 + t * 4, pk2(m0 + (float)k, m1 + (float)k));
    }
  }
  TS(13);
  bar();
#pragma unroll
  for (int i = 0; i < 8; ++i) st128(ln_mod(xv[i], 4096, i, lane, w), rw, lane * 16, WS_H + a * 32768 + ((w & 1) * 16 + (w >> 1) * 8 + i) * 1024);
  VMWAIT(0);
  bar();
  if (t == 0) st32(mktag(tag), rw, 0, CT_FH + a * 128);
  TS(5);
  mod_slice<4>(p, tag, rw, 1024 + a * 32, t, lane, w, xv, -1);
  if (a >= 21) {
#pragma unroll
    for (int m = 0; m < 3; ++m) {
      const u32x4* src = (const u32x4*)(m == 0 ? p.wg : (m == 1 ? p.wu : p.wd)) + (a - 21) * 2048 + t;
#pragma unroll
      for (int i = 0; i < 8; ++i) xv[i] = src[i * 256];
#pragma unroll
      for (int i = 0; i < 8; ++i) asm volatile("" :: "v"(xv[i]));
    }
  }
}

DEV void bg_ffn(const Params& p, u32 par, u32 tag, rsrc_t rw, int f, int t, int lane, int w) {
  const int c = lane & 15, g = lane >> 4;
  const bool up = f < 43;
  if (f >= 59) return;
  u32x8 W1[12];
  bar();
  if (up) {
#pragma unroll 1
    for (int i = 0; i < 8; ++i)
#pragma unroll
      for (int j = 0; j < 2; ++j) {
        const int I = 2 * w + j, sg = I * 8 + (lane >> 3), tl = ((lane >> 1) & 3) ^ swz<4>(sg);
        glds16(((tl >> 1) ? p.wu : p.wg) + (size_t)(i * 64 + rowmap<MAP_NAT>(sg)) * FF + f * 32 + (tl & 1) * 16 + (lane & 1) * 8, i * 8192 + I * 1024);
      }
    VMWAIT(0);
    bar();
#pragma unroll
    for (int i = 0; i < 8; ++i) W1[i] = slab_frag<4>(i * 8192, w, lane);
#pragma unroll
    for (int i = 8; i < 12; ++i) W1[i] = W1[0];
  } else {
#pragma unroll 1
    for (int i = 0; i < 6; ++i)
#pragma unroll
      for (int j = 0; j < 4; ++j) {
        const int sg = j * 16 + (lane >> 2), kr = min((w * 6 + i) * 64 + rowmap<MAP_NAT>(sg), FF - 1);
        glds16(p.wd + (size_t)kr * DM + (f - 43) * 32 + (lane & 3) * 8, (w * 6 + i) * 4096 + j * 1024);
      }
    VMWAIT(0);
    bar();
#pragma unroll
    for (int i = 0; i < 12; ++i) W1[i] = slab_frag<2>((w * 6 + (i >> 1)) * 4096, i & 1, lane);
  }
  bar();
  TS(12);
  const rsrc_t ro = mkrsrc(p.out, NROW * DM * 2);
  const int tok = lane & 1, d0 = max(f - 43, 0) * 32;
  for (u32 sp = 0; sp < SPINCAP && ld32(rw, 0, CT_TOP + (16 + (f & 15)) * 128) == SENTM; ++sp) { asm volatile("" ::: "memory"); __builtin_amdgcn_s_sleep(64); }
  asm volatile("" ::: "memory");
  const u32 gbv = ld32(rw, (d0 + (lane & 30)) * 4, WS_MOD + 5 * 2048);
  u64 p0 = 0, p1 = 0;
  float oval = 0.f;
#pragma unroll 1
  for (int rnd = 0; rnd < 2; ++rnd) {
    u32x4 ra, rb = {0, 0, 0, 0};
    for (u32 sp = 0; sp < SPINCAP && ld32(rw, 0, CT_TOP + rnd * 16384 + ((rnd ? LW1 : 80) + (f & 15)) * 128) == SENTM; ++sp) { asm volatile("" ::: "memory"); __builtin_amdgcn_s_sleep(3); }
    for (u32 sp = 0; sp < SPINCAP; ++sp) {
      asm volatile("" ::: "memory");
      ra = ld128(rw, lane * 128, CT_TOP + rnd * 16384);
      if (lane < 32) rb = ld128(rw, lane * 128, CT_TOP + rnd * 16384 + 8192);
      if (!__any(max(max(max(ra[0], ra[1]), max(ra[2], ra[3])), max(max(rb[0], rb[1]), max(rb[2], rb[3]))) == SENTM)) break;
      __builtin_amdgcn_s_sleep(2);
    }
    u64 k0 = ((u64)ra[1] << 32) | ra[0], k1 = ((u64)ra[3] << 32) | ra[2];
    top2_push(k0, k1, ((u64)rb[1] << 32) | rb[0]); top2_push(k0, k1, ((u64)rb[3] << 32) | rb[2]);
    TS(6);
    if (rnd) {
      u64 m = __ballot((k0 > p1 && k0 != p0) || k1 > p1);
      if (!m) break;
      u64 n0 = p0, n1 = p1;
      for (; m; m &= m - 1) {
        const int l = __builtin_ctzll(m);
        const u64 c0 = ((u64)(u32)__builtin_amdgcn_readlane((int)(k0 >> 32), l) << 32) | (u32)__builtin_amdgcn_readlane((int)(u32)k0, l);
        const u64 c1 = ((u64)(u32)__builtin_amdgcn_readlane((int)(k1 >> 32), l) << 32) | (u32)__builtin_amdgcn_readlane((int)(u32)k1, l);
        if ((u32)c0 != (u32)p0 && (u32)c0 != (u32)p1) top2_push(n0, n1, c0);
        if ((u32)c1 != (u32)p0 && (u32)c1 != (u32)p1) top2_push(n0, n1, c1);
      }
      k0 = n0; k1 = n1;
    } else {
#pragma unroll
      for (int m = 1; m < 64; m <<= 1) {
        const u64 o0 = shx(k0, m), o1 = shx(k1, m);
        const u64 lo = o0 < k0 ? o0 : k0, hi2 = o1 > k1 ? o1 : k1; k0 = o0 < k0 ? k0 : o0; k1 = lo > hi2 ? lo : hi2;
      }
    }
    p0 = k0; p1 = k1;
    TS(7);
    const int idx0 = (0x7FFFFFFF - (int)(u32)k0) & 2047, idx1 = (0x7FFFFFFF - (int)(u32)k1) & 2047;
    if (up) {
      if (w < 2) lds_wr128(4096 + rnd * 2304 + w * 1152 + lane * 16, ld128(rw, lane * 16, WS_H2 + rowoff(par, w ? idx1 : idx0)));
      bar();
      f32x4 d = {0, 0, 0, 0};
#pragma unroll
      for (int ks = 0; ks < 16; ++ks) d = mfma32(lds_rd128(4096 + rnd * 2304 + tok * 1152 + g * 16 + ks * 64), half4(W1[ks >> 1], ks & 1), d);
      if (w >= 2 && g == 0) lds_wr64(rnd * 256 + (w & 1) * 128 + c * 8, (u32x2){__float_as_uint(d[0]), __float_as_uint(d[1])});
      bar();
      if (w < 2) {
        const u32x2 uv = lds_rd64(rnd * 256 + w * 128 + c * 8);
        float av[2];
#pragma unroll
        for (int j = 0; j < 2; ++j) { const float gs = bfr(d[j]); av[j] = bfr(gs / (1.f + __expf(-gs))) * bfr(__uint_as_float(uv[j])); }
        const float n0 = __shfl_xor(av[0], 1), n1 = __shfl_xor(av[1], 1);
        const u32 v = tok ? pk2(n1, av[1]) : pk2(av[0], n0);
        if (g == 0) st32(nosent(v), rw, f * 128 + tok * 64 + (w * 16 + (c & 14)) * 2, WS_ACT + rnd * 6144);
      }
    } else {
      u32 hx = 0, xx = 0;
      if (w == 0) { const u32 o = rowoff(par, (lane >> 5) ? idx1 : idx0) + (d0 + (lane & 30)) * 2; hx = ld32(rw, o, WS_H2); xx = ld32(rw, o, WS_X1); }
      f32x4 d[2];
      __builtin_amdgcn_s_sleep(ACTSLEEP);
      TS(8);
      for (u32 sp = 0; sp < SPINCAP; ++sp) {
        asm volatile("" ::: "memory");
        u32x4 af[12];
#pragma unroll
        for (int ks = 0; ks < 12; ++ks) af[ks] = ld128(rw, tok * 64 + g * 16 + ks * 128, WS_ACT + rnd * 6144 + w * 1536);
        d[0] = (f32x4){0, 0, 0, 0}; d[1] = d[0];
        u32 mx = 0;
#pragma unroll
        for (int ks = 0; ks < 12; ++ks) {
          d[0] = mfma32(af[ks], half4(W1[(ks >> 1) * 2], ks & 1), d[0]); d[1] = mfma32(af[ks], half4(W1[(ks >> 1) * 2 + 1], ks & 1), d[1]);
          mx = max(max(mx, max(af[ks][0], af[ks][1])), max(af[ks][2], af[ks][3]));
        }
        if (!__any(mx == SENTM)) break;
        __builtin_amdgcn_s_sleep(2);
      }
      if (g == 0) {
#pragma unroll
        for (int i = 0; i < 4; ++i) lds_wr32(1024 + rnd * 1024 + ((i & 1) * 32 + (i >> 1) * 16 + c) * 16 + w * 4, __float_as_uint(d[i >> 1][i & 1]));
      }
      bar();
      if (w == 0) {
        const u32x4 v = lds_rd128(1024 + rnd * 1024 + lane * 16);
        const float y0 = bfr((__uint_as_float(v[0]) + __uint_as_float(v[1])) + (__uint_as_float(v[2]) + __uint_as_float(v[3])));
        const u32 ob = (u32)(((lane >> 5) ? k1 : k0) >> 32);
        const float rb = bfr(__uint_as_float(ob == 0 ? 0x7FC00000u : ((ob & 0x80000000u) ? (ob & 0x7FFFFFFFu) : ~ob)));
        const float y = bfr(y0 * bfr(1.f / (1.f + __expf(-rb))));
        const float h2 = (c & 1) ? bfhi(hx) : bflo(hx), x1 = (c & 1) ? bfhi(xx) : bflo(xx);
        oval = x1 + bfr(((c & 1) ? bfhi(gbv) : bflo(gbv)) * bfr(h2 + y));
      }
    }
  }
  TS(13);
  if (!up && w == 0) {
    const u32 row = (0x7FFFFFFF - (int)(u32)((lane >> 5) ? p1 : p0)) & 2047;
    __builtin_amdgcn_raw_buffer_store_b16((u16)pk2(oval, 0.f), ro, row * 1024 + (d0 + (lane & 31)) * 2, 0, SC1);
    if (f == 43 && lane == 0) st32(mktag(tag), rw, 0, CT_EPOCH);
  }
}

__global__ void __launch_bounds__(256, 1) k_main(Params p) {
  extern __shared__ __attribute__((aligned(16))) char smem[];
  const int b = blockIdx.x, t = threadIdx.x, lane = t & 63, w = __builtin_amdgcn_readfirstlane(t >> 6);
  const rsrc_t rw = mkrsrc(p.ws, WS_END);
  if (b >= 128) {
    u64 pc; asm volatile("s_getpc_b64 %0" : "=s"(pc));
    if (PFMODE & 1) glds16((const char*)(pc & ~(u64)255) + ((b >> 3) - 16) * 4096 + t * 16, 122880 + w * 1024);
  }
  TS(0);
  if ((b & 7) < 4 && b < 128) role_rec(p, b, t, lane, w);
  const u32 tag = ld32(rw, 0, CT_EPOCH);
  const int j = b & 3, i = b >> 3, hp = j * 4 + (i & 3);
  int grp = -1, f = 64;
  u32 par = 0;
  if ((b & 7) < 4) {
    role_init(p, rw, tag, j * 16 + i - 16, t, lane, w);
    bar();
    if (i >= 24) { f = j * 8 + i - 24; par = wait_start<false>(rw, tag, lane); }
  } else if (i < 16) {
    const int h = j * 8 + (i & 7);
    par = bg_proj<0, 32>(p, tag, rw, p.wk + h * DH, p.wv + h * DH, h, i >> 3, 2, CT_FKV + (h * 2 + (i >> 3)) * 128, t, lane, w);
    if (i < 12) grp = 3 + (i >> 2); else { f = 32 + j * 4 + i - 12; if (f >= 43) f = 64; }
  } else {
    role_aux(p, tag, rw, (i - 16) * 4 + j, t, lane, w);
    bar();
    if (i < 20) { par = bg_proj<1, 64>(p, tag, rw, p.wq + 2 * hp * DH, p.wq + (2 * hp + 1) * DH, 2 * hp, 0, 1, CT_FQ + hp * 128, t, lane, w); f = 43 + hp; }
    else grp = (i - 20) >> 2;
  }
  if (grp >= 0) bg_att(p, tag, rw, hp, grp, t, lane, w);
  else bg_ffn(p, par, tag, rw, f, t, lane, w);
  TS(14);
}
}

static const int g_dlya = DLY_A, g_dlyb = DLY_B;
extern "C" void kernel_launch(void* const* d_in, const int* in_sizes, int n_in, void* d_out, int out_size, void* d_ws, size_t ws_size,
                              hipStream_t stream) {
  Params p;
  p.x = (const u16*)d_in[0]; p.mem0 = (const u16*)d_in[1]; p.wq = (const u16*)d_in[2]; p.wk = (const u16*)d_in[3];
  p.wv = (const u16*)d_in[4]; p.wo = (const u16*)d_in[5]; p.wr = (const u16*)d_in[8]; p.wg = (const u16*)d_in[9];
  p.wu = (const u16*)d_in[10]; p.wd = (const u16*)d_in[11]; p.c = (const u16*)d_in[12]; p.wmod = (const u16*)d_in[13];
  p.out = (u16*)d_out;
  p.ws = (char*)(((uintptr_t)d_ws + PAGE - 1) & ~(uintptr_t)(PAGE - 1));
  p.dlya = g_dlya; p.dlyb = g_dlyb;
  (void)hipFuncSetAttribute((const void*)k_main, hipFuncAttributeMaxDynamicSharedMemorySize, LDS_BYTES);
  k_main<<<256, 256, LDS_BYTES, stream>>>(p);
}
```
